```python
import torch

def _softmax(x, axis=-1):
    return torch.softmax(x, dim=axis)
def _gelu(x):
    return torch.nn.functional.gelu(x, approximate="tanh")
def _silu(x):
    return torch.nn.functional.silu(x)
def _sigmoid(x):
    return torch.sigmoid(x)
def _layernorm(x, axis=-1, eps=1e-5):
    m = x.mean(dim=axis, keepdim=True)
    v = x.var(dim=axis, keepdim=True, unbiased=False)
    return (x - m) / torch.sqrt(v + eps)
def _scatter_add(tgt, idx, src, axis=0):
    return tgt.clone().index_add_(axis, idx, src)
def _segment_sum(src, seg, n):
    out = torch.zeros((n,) + src.shape[1:], dtype=src.dtype)
    return out.index_add_(0, seg, src)
def _pad(x, pw):
    flat = tuple(v for pair in reversed(pw) for v in pair)
    return torch.nn.functional.pad(x, flat)

kernel_name = 'interleave_layers__spectral_gfnet__softmax_full_mha__prompt'

def setup_inputs():

    _w = (lambda a: (lambda t: t.to(torch.bfloat16) if t.is_floating_point() else t)(torch.as_tensor(a)))
    def _A_setup():
        B, S, D = 1, 2048, 640
        Df = D // 2 + 1
        return {
            "x": _w((lambda *s: torch.randn(*s, dtype=torch.bfloat16) * 0.05)(B, S, D)),
            "w_r": _w((lambda *s: torch.randn(*s, dtype=torch.bfloat16) * 1.6)(S, Df) * 0.02),
            "w_i": _w((lambda *s: torch.randn(*s, dtype=torch.bfloat16) * 1.6)(S, Df) * 0.02),
        }
    _A_in = _A_setup()

    def _B_setup():
        B, S, D = 1, 2048, 640
        return {
            "x": _w((lambda *s: torch.randn(*s, dtype=torch.bfloat16) * 0.05)(B, S, D)),
            "wqkv": _w((lambda *s: torch.randn(*s, dtype=torch.bfloat16) * 0.05)(D, 3 * D)),
            "wo": _w((lambda *s: torch.randn(*s, dtype=torch.bfloat16) * 0.05)(D, D)),
        }
    _B_in = _B_setup()

    return {
        "A_x": _A_in["x"],
        "A_w_r": _A_in["w_r"],
        "A_w_i": _A_in["w_i"],
        "B_x": _B_in["x"],
        "B_wqkv": _B_in["wqkv"],
        "B_wo": _B_in["wo"],
    }

def reference(A_x, A_w_r, A_w_i, B_x, B_wqkv, B_wo):
    def _A_ref(x, w_r, w_i):
        B, S, D = x.shape
        X = (lambda x, axes=(-2,-1): torch.fft.rfft2(x.float(), dim=axes))(x, axes=(-2, -1))
        W = w_r + 1j * w_i
        X = X * W
        y = (lambda x, s, axes=(-2,-1): torch.fft.irfft2(x, s=s, dim=axes).to(torch.bfloat16))(X, (S, D), axes=(-2, -1))
        return y

    def _B_ref(x, wqkv, wo):
        B, S, D = x.shape
        H = 32; Dh = D // H
        qkv = (lambda x, *d: x.permute(*d))((x @ wqkv).reshape(B, S, 3, H, Dh), 2, 0, 3, 1, 4)
        q, k, v = qkv[0], qkv[1], qkv[2]
        scale = Dh ** -0.5
        scores = torch.einsum("bhqd,bhkd->bhqk", q, k) * scale
        p = _softmax(scores, axis=-1)
        o = torch.einsum("bhqk,bhkd->bhqd", p, v)
        return (lambda x, *d: x.permute(*d))(o, 0, 2, 1, 3).reshape(B, S, D) @ wo

    x = A_x
    x = x + _A_ref(_layernorm(x, axis=-1), A_w_r, A_w_i)
    x = x + _B_ref(_layernorm(x, axis=-1), B_wqkv, B_wo)
    x = x + _A_ref(_layernorm(x, axis=-1), A_w_r, A_w_i)
    x = x + _B_ref(_layernorm(x, axis=-1), B_wqkv, B_wo)
    return x
```

```cpp
#include <hip/hip_runtime.h>
#include <stdint.h>

typedef __attribute__((ext_vector_type(2))) __bf16 bf16x2;
typedef __attribute__((ext_vector_type(8))) __bf16 bf16x8;
typedef __attribute__((ext_vector_type(16))) __bf16 bf16x16;
typedef __attribute__((ext_vector_type(4))) short s16x4;
typedef __attribute__((ext_vector_type(2))) float f32x2;
typedef __attribute__((ext_vector_type(4))) float f32x4;
typedef __attribute__((ext_vector_type(8))) float f32x8;
typedef __attribute__((ext_vector_type(16))) float f32x16;
typedef __attribute__((ext_vector_type(2))) unsigned u32x2;
typedef __attribute__((ext_vector_type(4))) unsigned u32x4;
#define LAS __attribute__((address_space(3)))
#define GAS __attribute__((address_space(1)))
typedef LAS char* lds_ptr;
typedef GAS unsigned gu32;

#define SBAR() __builtin_amdgcn_sched_barrier(0)
#define PIN(x) asm volatile("" : "+v"(x))
#define WAITV(N) asm volatile("s_waitcnt vmcnt(%0)" ::"i"(N) : "memory")
#define WAIT_BAR(N) asm volatile("s_waitcnt vmcnt(%0) lgkmcnt(0)\n\ts_barrier" ::"i"(N) : "memory")
#define LBAR() asm volatile("s_waitcnt lgkmcnt(0)\n\ts_barrier" ::: "memory")
#define MFMA32(a, b, c) __builtin_amdgcn_mfma_f32_32x32x16_bf16(a, b, c, 0, 0, 0)
#define MFMA32K8(a, b, c) __builtin_amdgcn_mfma_f32_32x32x8bf16_1k(a, b, c, 0, 0, 0)
#define MFMA16(a, b, c) __builtin_amdgcn_mfma_f32_16x16x32_bf16(a, b, c, 0, 0, 0)
#define RLX_AGENT __ATOMIC_RELAXED, __HIP_MEMORY_SCOPE_AGENT

constexpr size_t SZ_X = 2048ull * 640 * 2;
constexpr size_t OFF_FLAGS = 0;
constexpr size_t OFF_SS0 = 65536;
constexpr size_t OFF_SS2 = OFF_SS0 + 16384;
constexpr size_t OFF_XA = OFF_SS2 + 16384;
constexpr size_t OFF_XB = OFF_XA + SZ_X;
constexpr size_t OFF_OB = OFF_XB + SZ_X;
constexpr size_t OFF_X3 = OFF_OB + SZ_X;
constexpr size_t OFF_WF = OFF_X3 + SZ_X;
constexpr size_t OFF_WQ = OFF_WF + SZ_X;
constexpr size_t OFF_FR = OFF_WQ + SZ_X;
constexpr size_t OFF_WO = OFF_FR + 819200;
constexpr size_t OFF_KV = OFF_WO + 819200;
constexpr int TILE_B = 2624;
constexpr size_t SZ_KV = 32ull * 64 * TILE_B;

__device__ __forceinline__ unsigned pack2(float a, float b) {
  f32x2 f = {a, b};
  return __builtin_bit_cast(unsigned, __builtin_convertvector(f, bf16x2));
}
__device__ __forceinline__ float lo16(unsigned u) { return __builtin_bit_cast(float, u << 16); }
__device__ __forceinline__ float hi16(unsigned u) { return __builtin_bit_cast(float, u & 0xffff0000u); }
__device__ __forceinline__ void glds16(const void* g, unsigned lds_base) {
  unsigned sv;
  asm volatile("s_mov_b32 %0, m0\n\ts_mov_b32 m0, %2\n\ts_nop 0\n\tglobal_load_lds_dwordx4 %1, off\n\ts_mov_b32 m0, %0"
               : "=&s"(sv) : "v"(g), "s"(lds_base) : "memory");
}
#define GLOAD16(dst, ptr, OFF) asm volatile("global_load_dwordx4 %0, %1, off offset:%2" : "=v"(dst) : "v"(ptr), "i"(OFF) : "memory")
__device__ __forceinline__ bf16x8 lds_rd16(lds_ptr p) { return *(const LAS bf16x8*)p; }
__device__ __forceinline__ s16x4 lds_tr(lds_ptr p) {
  return __builtin_amdgcn_ds_read_tr16_b64_v4i16((LAS s16x4*)p);
}
__device__ __forceinline__ bf16x8 cat8(s16x4 a, s16x4 b) {
  typedef __attribute__((ext_vector_type(8))) short s16x8;
  s16x8 r = {a[0], a[1], a[2], a[3], b[0], b[1], b[2], b[3]};
  return __builtin_bit_cast(bf16x8, r);
}

template <bool TR>
__device__ __forceinline__ void k0_gemm(const char* src, int row0, int cg, int kind, char* ws, lds_ptr smem) {
  const int tid = threadIdx.x, l = tid & 63, w = __builtin_amdgcn_readfirstlane(tid >> 6);
  const unsigned lds0 = (unsigned)(uintptr_t)smem;
  {
    const char* g0;
    const char* g1;
    int kstride;
    if (!TR) {
      const int r0 = 16 * w + (l >> 3);
      g0 = src + (size_t)(row0 + r0) * 1280 + (((l & 7) ^ ((l >> 4) & 7)) << 4);
      g1 = src + (size_t)(row0 + r0 + 8) * 1280 + (((l & 7) ^ ((4 + (l >> 4)) & 7)) << 4);
      kstride = 128;
    } else {
      const int n0 = 16 * w + (l >> 3);
      g0 = src + (size_t)n0 * 3840 + row0 * 2 + (((l & 7) ^ (((l >> 4) & 1) << 2)) << 4);
      g1 = g0 + 8 * 3840;
      kstride = 64 * 3840;
    }
#pragma unroll
    for (int kg = 0; kg < 10; ++kg) {
      glds16(g0 + (size_t)kg * kstride, lds0 + (kg * 8 + 2 * w) * 1024);
      glds16(g1 + (size_t)kg * kstride, lds0 + (kg * 8 + 2 * w + 1) * 1024);
    }
  }
  const int h = l >> 5, r32 = l & 31;
  const int cp = cg * 128 + 32 * w + r32;
  float nu = (float)(cp >> 1), amp = 0.05590169943749474f, ph = (cp & 1) ? 0.25f : 0.f;
  if (cp < 2) { amp = 0.03952847075210474f; ph = 0.f; nu = cp ? 0.f : 320.f; }
  float fa[8], fb[8];
#pragma unroll
  for (int j = 0; j < 8; ++j) {
    const float n = (float)(8 * h + j);
    fa[j] = amp * __builtin_amdgcn_cosf(__builtin_fmaf(nu * n, 1.f / 640.f, ph));
    fb[j] = amp * __builtin_amdgcn_cosf(__builtin_fmaf(nu * (n + 16.f), 1.f / 640.f, ph));
  }
  const float co = 2.f * __builtin_amdgcn_cosf(nu * (1.f / 40.f));
  lds_ptr ra[4];
  if (!TR) {
    const int f = (r32 >> 1) & 7;
#pragma unroll
    for (int q = 0; q < 4; ++q) ra[q] = smem + (r32 >> 3) * 1024 + (r32 & 7) * 128 + (((2 * q + h) ^ f) << 4);
  } else {
    const int gm = l >> 4, q = (l >> 2) & 3, p = l & 3;
#pragma unroll
    for (int R = 0; R < 2; ++R)
      ra[R] = smem + (8 * (gm >> 1) + q) * 128 + (((4 * R + 2 * (gm & 1) + (p >> 1)) ^ (((q >> 1) & 1) << 2)) << 4) + (p & 1) * 8;
    ra[2] = ra[3] = ra[0];
  }
  f32x16 acc0 = {}, acc1 = {};
#pragma unroll 1
  for (int st = 0; st < 5; ++st) {
    if (st == 0) WAIT_BAR(16); else if (st == 1) WAIT_BAR(12); else if (st == 2) WAIT_BAR(8); else if (st == 3) WAIT_BAR(4); else WAIT_BAR(0);
    const int so = st * 16384;
#pragma unroll
    for (int k8 = 0; k8 < 8; ++k8) {
      bf16x8 b0, b1;
      if (!TR) {
        b0 = lds_rd16(ra[k8 & 3] + so + (k8 >> 2) * 8192);
        b1 = lds_rd16(ra[k8 & 3] + so + (k8 >> 2) * 8192 + 4096);
      } else {
        b0 = cat8(lds_tr(ra[0] + so + k8 * 2048), lds_tr(ra[0] + so + k8 * 2048 + 512));
        b1 = cat8(lds_tr(ra[1] + so + k8 * 2048), lds_tr(ra[1] + so + k8 * 2048 + 512));
      }
      float* cur = (k8 & 1) ? fb : fa;
      float* nxt = (k8 & 1) ? fa : fb;
      u32x4 pk = {pack2(cur[0], cur[1]), pack2(cur[2], cur[3]), pack2(cur[4], cur[5]), pack2(cur[6], cur[7])};
      const bf16x8 a = __builtin_bit_cast(bf16x8, pk);
      acc0 = MFMA32(a, b0, acc0);
      acc1 = MFMA32(a, b1, acc1);
#pragma unroll
      for (int j = 0; j < 8; ++j) cur[j] = __builtin_fmaf(co, nxt[j], -cur[j]);
    }
  }
  const int c0 = cg * 128 + 32 * w + 4 * h;
#pragma unroll
  for (int R = 0; R < 2; ++R) {
    f32x16 acc = R ? acc1 : acc0;
    const int row = row0 + 32 * R + r32;
    char* base;
    if (TR) {
      const int which = row / 640, rem = row - which * 640, head = rem / 20, d = rem - head * 20;
      int T, rr;
      if (which == 0) { T = d >> 4; rr = d; }
      else if (which == 1) { T = d >> 4; rr = (d < 16) ? 16 + d : 4 + d; }
      else {
        int e = (head & 1) ? ((d < 4) ? d + 16 : d - 4) : d;
        T = 1;
        rr = (e < 16) ? ((e & 3) | ((e & 4) << 1) | ((e & 8) >> 1)) : e + 8;
      }
      base = ws + OFF_WQ + (size_t)head * 81920 + T * 1024 + rr * 16;
      if (c0 == 0) acc[1] = 0.f;
    } else if (kind == 0) {
      base = ws + OFF_XA + row * 8;
    } else {
      base = ws + OFF_WO + row * 1280;
    }
#pragma unroll
    for (int p = 0; p < 4; ++p) {
      const int c = c0 + 8 * p;
      size_t off;
      if (TR) off = (c >> 4) * 2048 + ((c >> 3) & 1) * 512 + ((c >> 2) & 1) * 8;
      else off = (kind == 0) ? (size_t)(c >> 2) * 16384 : (size_t)c * 2;
      u32x2 v = {pack2(acc[4 * p], acc[4 * p + 1]), pack2(acc[4 * p + 2], acc[4 * p + 3])};
      *(u32x2*)(base + off) = v;
    }
  }
}

__global__ __launch_bounds__(256, 2) void k_pre(const char* __restrict__ x, const char* __restrict__ wr, const char* __restrict__ wi,
                                                const char* __restrict__ wqkv, const char* __restrict__ wo, char* __restrict__ ws) {
  extern __shared__ __attribute__((aligned(16))) char smem_[];
  lds_ptr smem = (lds_ptr)smem_;
  const int bid = blockIdx.x, tid = threadIdx.x;
  if (bid < 160) {
    const int i = bid >> 3;
    k0_gemm<false>(x, ((bid & 7) * 4 + i / 5) * 64, i % 5, 0, ws, smem);
  } else if (bid < 288) {
    const int k0 = (bid - 160) * 16;
    const u32x4* s0 = (const u32x4*)(wr + (size_t)k0 * 642);
    const u32x4* s1 = (const u32x4*)(wi + (size_t)k0 * 642);
    u32x4 v0[3], v1[3];
#pragma unroll
    for (int i = 0; i < 3; ++i) {
      const int it = min(tid + 256 * i, 641);
      v0[i] = s0[it];
      v1[i] = s1[it];
    }
#pragma unroll
    for (int i = 0; i < 3; ++i) {
      const int it = min(tid + 256 * i, 641);
      *(LAS u32x4*)(smem + it * 16) = v0[i];
      *(LAS u32x4*)(smem + 10272 + it * 16) = v1[i];
    }
    __syncthreads();
    const LAS unsigned short* L = (const LAS unsigned short*)smem;
#pragma unroll
    for (int rd = 0; rd < 5; ++rd) {
      const int c = rd * 64 + (tid >> 2), i4 = (tid & 3) * 4;
      const int k2 = c ? c : 320;
      u32x4 o;
#pragma unroll
      for (int i = 0; i < 4; ++i) o[i] = (unsigned)L[(i4 + i) * 321 + k2] | ((unsigned)L[5136 + (i4 + i) * 321 + k2] << 16);
      *(u32x4*)(ws + OFF_WF + ((size_t)c * 2048 + k0 + i4) * 4) = o;
    }
  } else if (bid < 320) {
    const int b = bid - 288;
    const int l16 = tid & 15;
    u32x4 v[4][5];
#pragma unroll
    for (int i = 0; i < 4; ++i) {
      const int row = b * 64 + i * 16 + (tid >> 4);
#pragma unroll
      for (int j = 0; j < 5; ++j) v[i][j] = *(const u32x4*)(x + (size_t)row * 1280 + (j * 16 + l16) * 16);
    }
    *(u32x2*)(ws + OFF_FLAGS + (b * 256 + tid) * 8) = (u32x2){0u, 0u};
    if (tid < 64) *(double*)(ws + OFF_SS2 + (b * 64 + tid) * 8) = 0.0;
#pragma unroll
    for (int i = 0; i < 4; ++i) {
      float s = 0.f;
#pragma unroll
      for (int j = 0; j < 5; ++j)
#pragma unroll
        for (int e = 0; e < 4; ++e) s += lo16(v[i][j][e]) + hi16(v[i][j][e]);
#pragma unroll
      for (int m = 1; m < 16; m <<= 1) s += __shfl_xor(s, m);
      const float mean = s * (1.f / 640.f);
      float q = 0.f;
#pragma unroll
      for (int j = 0; j < 5; ++j)
#pragma unroll
        for (int e = 0; e < 4; ++e) {
          const float a = lo16(v[i][j][e]) - mean, c = hi16(v[i][j][e]) - mean;
          q += a * a + c * c;
        }
#pragma unroll
      for (int m = 1; m < 16; m <<= 1) q += __shfl_xor(q, m);
      if (l16 == 0) *(double*)(ws + OFF_SS0 + (b * 64 + i * 16 + (tid >> 4)) * 8) = (double)q;
    }
  } else if (bid < 470) {
    const int i = bid - 320;
    k0_gemm<true>(wqkv, (i / 5) * 64, i % 5, 1, ws, smem);
  } else {
    const int i = bid - 470;
    k0_gemm<false>(wo, (i / 5) * 64, i % 5, 2, ws, smem);
  }
}

__device__ __forceinline__ void cs_pair(float rev, unsigned& re, unsigned& im) {
  const float c = __builtin_amdgcn_cosf(rev), s = __builtin_amdgcn_sinf(rev);
  re = pack2(c, s);
  im = pack2(-s, c);
}

__global__ __launch_bounds__(256) void k_col(char* __restrict__ ws, int layer) {
  __shared__ __attribute__((aligned(16))) char smem_[32768];
  lds_ptr smem = (lds_ptr)smem_;
  const int bid = blockIdx.x, tid = threadIdx.x;
  if (bid >= 160) {
    for (int it = (bid - 160) * 256 + tid; it < 51200; it += 96 * 256) {
      const int row = it % 20, ch = (it / 20) % 80, head = it / 1600;
      const int n = head * 20 + row;
      u32x4 o;
#pragma unroll
      for (int i = 0; i < 4; ++i) {
        const int c = ch * 4 + i;
        const float rev = (float)((c * n) % 640) * (1.f / 640.f);
        float a = 0.05590169943749474f * __builtin_amdgcn_cosf(rev), b = -0.05590169943749474f * __builtin_amdgcn_sinf(rev);
        if (c == 0) { b = 0.03952847075210474f; a = (n & 1) ? -b : b; }
        o[i] = pack2(a, b);
      }
      *(u32x4*)(ws + OFF_FR + (size_t)it * 16) = o;
    }
    return;
  }
  const int u = ((bid & 7) * 20 + (bid >> 3));
  const int l = tid & 63, w = __builtin_amdgcn_readfirstlane(tid >> 6), p = l & 15, g = l >> 4;
  const int srow = 32 * (16 * w + 4 * g) + 2 * p;
  u32x4 xr[4];
  double2 ssv[4];
  const char* ssp = ws + (layer ? OFF_SS2 : OFF_SS0);
#pragma unroll
  for (int i = 0; i < 4; ++i) {
    xr[i] = *(const u32x4*)(ws + OFF_XA + (size_t)u * 16384 + (srow + 32 * i) * 8);
    ssv[i] = *(const double2*)(ssp + (srow + 32 * i) * 8);
  }
  unsigned wf[2][8];
#pragma unroll
  for (int c = 0; c < 2; ++c)
#pragma unroll
    for (int i = 0; i < 8; ++i)
      wf[c][i] = *(const unsigned*)(ws + OFF_WF + ((size_t)(2 * u + c) * 2048 + 16 * w + p + 64 * (16 * (i >> 2) + 4 * g + (i & 3))) * 4);
  u32x4 b1r[4], b1i[4], a2r[2][2], a2i[2][2];
  float twr[8], twi[8];
  {
    const float m = (float)(16 * w + p);
#pragma unroll
    for (int k = 0; k < 4; ++k)
#pragma unroll
      for (int i = 0; i < 4; ++i) {
        unsigned re, im;
        cs_pair(m * (float)(16 * k + 4 * g + i) * (1.f / 64.f), re, im);
        b1r[k][i] = re; b1i[k][i] = im;
      }
#pragma unroll
    for (int t2 = 0; t2 < 2; ++t2)
#pragma unroll
      for (int t = 0; t < 2; ++t)
#pragma unroll
        for (int i = 0; i < 4; ++i) {
          unsigned re, im;
          cs_pair((float)(16 * t2 + p) * (float)(16 * t + 4 * g + i) * (1.f / 32.f), re, im);
          a2r[t2][t][i] = re; a2i[t2][t][i] = im;
        }
#pragma unroll
    for (int i = 0; i < 8; ++i) {
      const float rev = m * (float)(16 * (i >> 2) + 4 * g + (i & 3)) * (1.f / 2048.f);
      twr[i] = __builtin_amdgcn_cosf(rev);
      twi[i] = -__builtin_amdgcn_sinf(rev);
    }
  }
  const float mask0 = (u == 0) ? 0.f : 1.f;
  {
    u32x4 z[2][2];
#pragma unroll
    for (int i = 0; i < 4; ++i) {
      const float r0 = __builtin_amdgcn_rsqf(__builtin_fmaf((float)ssv[i].x, 1.f / 640.f, 1e-5f));
      const float r1 = __builtin_amdgcn_rsqf(__builtin_fmaf((float)ssv[i].y, 1.f / 640.f, 1e-5f));
      z[0][0][i] = pack2(lo16(xr[i][0]) * r0, hi16(xr[i][0]) * (r0 * mask0));
      z[1][0][i] = pack2(lo16(xr[i][1]) * r0, hi16(xr[i][1]) * r0);
      z[0][1][i] = pack2(lo16(xr[i][2]) * r1, hi16(xr[i][2]) * (r1 * mask0));
      z[1][1][i] = pack2(lo16(xr[i][3]) * r1, hi16(xr[i][3]) * r1);
    }
#pragma unroll
    for (int c = 0; c < 2; ++c)
#pragma unroll
      for (int e = 0; e < 2; ++e) {
        const int n2 = 2 * p + e;
        *(LAS u32x4*)(smem + c * 8192 + n2 * 256 + (((4 * w + g) ^ (n2 & 15)) << 4)) = z[c][e];
      }
  }
  __syncthreads();
#pragma unroll
  for (int c = 0; c < 2; ++c) {
    f32x4 tr[2] = {}, ti[2] = {};
#pragma unroll
    for (int t = 0; t < 2; ++t)
#pragma unroll
      for (int k = 0; k < 4; ++k) {
        const bf16x8 a = lds_rd16(smem + c * 8192 + (16 * t + p) * 256 + (((4 * k + g) ^ p) << 4));
        tr[t] = MFMA16(a, __builtin_bit_cast(bf16x8, b1r[k]), tr[t]);
        ti[t] = MFMA16(a, __builtin_bit_cast(bf16x8, b1i[k]), ti[t]);
      }
    u32x4 bb[2];
#pragma unroll
    for (int i = 0; i < 8; ++i) {
      const float a = tr[i >> 2][i & 3], b = ti[i >> 2][i & 3];
      bb[i >> 2][i & 3] = pack2(a * twr[i] - b * twi[i], a * twi[i] + b * twr[i]);
    }
    f32x4 xr2[2] = {}, xi2[2] = {};
#pragma unroll
    for (int t2 = 0; t2 < 2; ++t2)
#pragma unroll
      for (int t = 0; t < 2; ++t) {
        xr2[t2] = MFMA16(__builtin_bit_cast(bf16x8, a2r[t2][t]), __builtin_bit_cast(bf16x8, bb[t]), xr2[t2]);
        xi2[t2] = MFMA16(__builtin_bit_cast(bf16x8, a2i[t2][t]), __builtin_bit_cast(bf16x8, bb[t]), xi2[t2]);
      }
#pragma unroll
    for (int i = 0; i < 8; ++i) {
      const float a = xr2[i >> 2][i & 3], b = xi2[i >> 2][i & 3], fr = lo16(wf[c][i]), fi = hi16(wf[c][i]);
      bb[i >> 2][i & 3] = pack2(a * fr - b * fi, -(a * fi + b * fr));
    }
    f32x4 ur[2] = {}, ui[2] = {};
#pragma unroll
    for (int t2 = 0; t2 < 2; ++t2)
#pragma unroll
      for (int t = 0; t < 2; ++t) {
        ur[t2] = MFMA16(__builtin_bit_cast(bf16x8, a2r[t2][t]), __builtin_bit_cast(bf16x8, bb[t]), ur[t2]);
        ui[t2] = MFMA16(__builtin_bit_cast(bf16x8, a2i[t2][t]), __builtin_bit_cast(bf16x8, bb[t]), ui[t2]);
      }
#pragma unroll
    for (int i = 0; i < 8; ++i) {
      const float a = ur[i >> 2][i & 3], b = ui[i >> 2][i & 3];
      const int n2 = 16 * (i >> 2) + 4 * g + (i & 3);
      const int rw = (n2 >> 1) + 16 * (n2 & 1);
      *(LAS unsigned*)(smem + 16384 + c * 8192 + rw * 256 + (((4 * w + (p >> 2)) ^ (rw & 15)) << 4) + (p & 3) * 4) =
          pack2(a * twr[i] - b * twi[i], a * twi[i] + b * twr[i]);
    }
  }
  __syncthreads();
  f32x4 yr[2][2] = {}, yi[2][2] = {};
#pragma unroll
  for (int c = 0; c < 2; ++c)
#pragma unroll
    for (int t = 0; t < 2; ++t)
#pragma unroll
      for (int k = 0; k < 4; ++k) {
        const bf16x8 b = lds_rd16(smem + 16384 + c * 8192 + (16 * t + p) * 256 + (((4 * k + g) ^ p) << 4));
        yr[c][t] = MFMA16(__builtin_bit_cast(bf16x8, b1r[k]), b, yr[c][t]);
        yi[c][t] = MFMA16(__builtin_bit_cast(bf16x8, b1i[k]), b, yi[c][t]);
      }
  const float sc = 1.f / 2048.f, sc0 = -sc * mask0;
  char* ob = ws + OFF_XB + (size_t)(16 * w + 4 * g) * 40960 + (u >> 1) * 512 + (2 * p) * 16 + (u & 1) * 8;
#pragma unroll
  for (int r = 0; r < 4; ++r)
#pragma unroll
    for (int t = 0; t < 2; ++t) {
      const unsigned x0 = xr[r][2 * t], x1 = xr[r][2 * t + 1];
      u32x2 o = {pack2(__builtin_fmaf(yr[0][t][r], sc, lo16(x0)), __builtin_fmaf(yi[0][t][r], sc0, hi16(x0))),
                 pack2(__builtin_fmaf(yr[1][t][r], sc, lo16(x1)), __builtin_fmaf(yi[1][t][r], -sc, hi16(x1)))};
      *(u32x2*)(ob + (size_t)r * 40960 + t * 16) = o;
    }
}

constexpr int POS_B = 4 * TILE_B;
constexpr int ATT_LDS = 15 * POS_B;
__device__ __forceinline__ constexpr int slot_off(int pos) { return ((pos + 13) % 15) * POS_B; }
#define SMF(a, b, c) __builtin_amdgcn_smfmac_f32_32x32x32_bf16(a, __builtin_bit_cast(bf16x16, b), c, sidx, 0, 0)
#define EXP2(x) __builtin_amdgcn_exp2f(x)

template <int LAYER>
__global__ __launch_bounds__(512, 2) void k_attn(char* __restrict__ ws) {
  extern __shared__ __attribute__((aligned(16))) char smem_[];
  lds_ptr smem = (lds_ptr)smem_;
  const unsigned lds0 = (unsigned)(uintptr_t)smem;
  const int bid = blockIdx.x, tid = threadIdx.x, l = tid & 63, w = __builtin_amdgcn_readfirstlane(tid >> 6);
  const int h = l >> 5, r32 = l & 31;
  const int head = ((bid & 7) >> 1) * 8 + ((bid >> 3) & 7), qb = (bid & 1) * 4 + (bid >> 6);
  {
    const char* g = ws + OFF_WQ + (size_t)head * 81920 + w * 1024 + l * 16;
#pragma unroll
    for (int i = 0; i < 10; ++i) glds16(g + i * 8192, lds0 + i * 8192 + w * 1024);
    if (LAYER) {
      const char* f = ws + OFF_FR + (size_t)head * 25600 + l * 16;
#pragma unroll
      for (int i = 0; i < 3; ++i) glds16(f + (i * 8 + w) * 1024, lds0 + 81920 + (i * 8 + w) * 1024);
      glds16(f + 24 * 1024, lds0 + 81920 + 24 * 1024);
    }
  }
  constexpr int WIN = 16;
  const char* xp = ws + OFF_XB + (size_t)(qb * 8 + w) * 40960 + l * 16;
  u32x4 xf[WIN];
#pragma unroll
  for (int i = 0; i < WIN; ++i) GLOAD16(xf[i], xp + (i >> 2) * 4096, (i & 3) * 1024);
  WAIT_BAR(WIN);
  f32x16 t0 = {}, t1 = {}, t2 = {};
  float ssq = 0.f;
  {
    const lds_ptr wa = smem + l * 16;
    const lds_ptr fa = smem + 81920 + (h * 20 + (r32 < 20 ? r32 : (r32 < 28 ? 0 : 16))) * 16;
#pragma unroll
    for (int ks = 0; ks < 40; ++ks) {
      const bf16x8 a0 = lds_rd16(wa + ks * 2048), a1 = lds_rd16(wa + ks * 2048 + 1024);
      bf16x8 a2;
      if (LAYER) a2 = lds_rd16(fa + ks * 640);
      WAITV((ks + WIN < 40) ? WIN - 1 : 39 - ks);
      PIN(xf[ks % WIN]);
      const u32x4 xv = xf[ks % WIN];
      const bf16x8 b = __builtin_bit_cast(bf16x8, xv);
      t0 = MFMA32(a0, b, t0);
      t1 = MFMA32(a1, b, t1);
      if (LAYER) t2 = MFMA32(a2, b, t2);
#pragma unroll
      for (int e = 0; e < 4; ++e) {
        unsigned v = xv[e];
        if (ks == 0 && e == 0) v &= h ? 0xffffffffu : 0x0000ffffu;
        ssq = __builtin_amdgcn_fdot2_f32_bf16(__builtin_bit_cast(bf16x2, v), __builtin_bit_cast(bf16x2, v), ssq, false);
      }
      SBAR();
      if (ks + WIN < 40) GLOAD16(xf[ks % WIN], xp + ((ks + WIN) >> 2) * 4096, ((ks + WIN) & 3) * 1024);
    }
  }
  ssq += __shfl_xor(ssq, 32);
  const float rs = __builtin_amdgcn_rsqf(__builtin_fmaf(ssq, 1.f / 640.f, 1e-5f));
  const float qs = rs * (0.22360679774997896f * 1.4426950408889634f);
  u32x4 q1u = {pack2(t0[0] * qs, t0[1] * qs), pack2(t0[2] * qs, t0[3] * qs), pack2(t0[4] * qs, t0[5] * qs), pack2(t0[6] * qs, t0[7] * qs)};
  const float s2 = h ? rs : qs;
  u32x2 e2 = {pack2(t1[8] * s2, t1[9] * s2), pack2(t1[10] * s2, t1[11] * s2)};
  u32x2 q2u = {h ? 0u : e2[0], h ? 0u : e2[1]};
  const bf16x8 q1 = __builtin_bit_cast(bf16x8, q1u);
  const s16x4 q2 = __builtin_bit_cast(s16x4, q2u);
  const lds_ptr mine = smem + slot_off(0) + w * TILE_B;
  {
    u32x4 k1u = {pack2(t0[8] * rs, t0[9] * rs), pack2(t0[10] * rs, t0[11] * rs), pack2(t0[12] * rs, t0[13] * rs), pack2(t0[14] * rs, t0[15] * rs)};
    *(LAS u32x4*)(mine + l * 16) = k1u;
    if (h) *(LAS u32x2*)(mine + 1024 + r32 * 8) = e2;
    const lds_ptr vb = mine + 1280 + (r32 >> 3) * 336 + (r32 & 7) * 2;
#pragma unroll
    for (int r = 0; r < 8; r += 2) {
      const unsigned pv = pack2(t1[r] * rs, t1[r + 1] * rs);
      const int sg = (r & 3) + 8 * (r >> 2);
      *(LAS unsigned short*)(vb + (sg + 4 * h) * 16) = (unsigned short)pv;
      *(LAS unsigned short*)(vb + (sg + 1 + 4 * h) * 16) = (unsigned short)(pv >> 16);
    }
    if (!h) {
#pragma unroll
      for (int r = 12; r < 16; r += 2) {
        const unsigned pv = pack2(t1[r] * rs, t1[r + 1] * rs);
        *(LAS unsigned short*)(vb + (r + 4) * 16) = (unsigned short)pv;
        *(LAS unsigned short*)(vb + (r + 5) * 16) = (unsigned short)(pv >> 16);
      }
    } else {
      *(LAS unsigned short*)(vb + 20 * 16) = (unsigned short)0x3f80;
    }
  }
  char* kvh = ws + OFF_KV + LAYER * SZ_KV + (size_t)head * 64 * TILE_B;
  {
    const __amdgpu_buffer_rsrc_t rsrc = __builtin_amdgcn_make_buffer_rsrc(kvh + (size_t)(qb * 8 + w) * TILE_B, 0, TILE_B, 0x00020000);
    asm volatile("s_waitcnt lgkmcnt(0)" ::: "memory");
#pragma unroll
    for (int i = 0; i < 3; ++i) {
      const int off = min(i * 1024 + l * 16, TILE_B - 16);
      const u32x4 v = *(const LAS u32x4*)(mine + off);
      __builtin_amdgcn_raw_buffer_store_b128(v, rsrc, i * 1024 + l * 16, 0, 16);
    }
  }
  if (LAYER) {
    char* x3 = ws + OFF_X3 + (size_t)(5 * head + h) * 16384 + (qb * 256 + w * 32 + r32) * 8;
    *(u32x2*)(x3) = (u32x2){pack2(t2[0], t2[1]), pack2(t2[2], t2[3])};
    *(u32x2*)(x3 + 2 * 16384) = (u32x2){pack2(t2[4], t2[5]), pack2(t2[6], t2[7])};
    if (!h) *(u32x2*)(x3 + 4 * 16384) = (u32x2){pack2(t2[8], t2[9]), pack2(t2[10], t2[11])};
  }
  LBAR();
  const int k1o = l * 16, k2o = 1024 + r32 * 8, vo = 1280 + (h * 21 + (r32 < 20 ? r32 : (r32 < 28 ? 20 : 16))) * 16;
  unsigned sidx = 0xDDDDDDDDu;
  PIN(sidx);
  f32x16 o = {}, sA, sB, negm;
  f32x8 pl, ph;
  bf16x8 kA1, kB1, v0, v1;
  s16x4 kA2, kB2;
  lds_ptr k1p = smem + slot_off(0) + k1o, k2p = smem + slot_off(0) + k2o, vcur = smem + slot_off(0) + vo, vnext = vcur;
#define RDK(K1, K2, i) do { K1 = lds_rd16(k1p + (i) * TILE_B); K2 = *(const LAS s16x4*)(k2p + (i) * TILE_B); } while (0)
  RDK(kA1, kA2, 0);
  RDK(kB1, kB2, 1);
  v0 = lds_rd16(vcur);
  {
    f32x16 z = {};
    sA = MFMA32K8(kA2, q2, z);
    sA = MFMA32(kA1, q1, sA);
    float m = fmaxf(sA[0], sA[1]);
#pragma unroll
    for (int r = 2; r < 16; ++r) m = fmaxf(m, sA[r]);
    m = fmaxf(m, __shfl_xor(m, 32));
#pragma unroll
    for (int r = 0; r < 16; ++r) { negm[r] = -m; sA[r] -= m; }
    PIN(negm);
  }
#define PERIOD(SC, SN, KC1, KC2, KN1, KN2, KI, VP1, VP0, FIRST)                                        \
  do {                                                                                                  \
    SN = MFMA32K8(KN2, q2, negm);                                                                       \
    pl[0] = EXP2(SC[0]); pl[1] = EXP2(SC[1]); pl[2] = EXP2(SC[2]); pl[3] = EXP2(SC[3]); PIN(pl); SBAR(); \
    SN = MFMA32(KN1, q1, SN);                                                                           \
    RDK(KC1, KC2, KI);                                                                                  \
    pl[4] = EXP2(SC[4]); pl[5] = EXP2(SC[5]); pl[6] = EXP2(SC[6]); pl[7] = EXP2(SC[7]); PIN(pl); SBAR(); \
    if (!(FIRST)) o = SMF(v1, ph, o);                                                                   \
    SBAR();                                                                                             \
    v1 = lds_rd16(VP1);                                                                                 \
    ph[0] = EXP2(SC[8]); ph[1] = EXP2(SC[9]); ph[2] = EXP2(SC[10]); ph[3] = EXP2(SC[11]); PIN(ph); SBAR(); \
    o = SMF(v0, pl, o);                                                                                 \
    SBAR();                                                                                             \
    v0 = lds_rd16(VP0);                                                                                 \
    ph[4] = EXP2(SC[12]); ph[5] = EXP2(SC[13]); ph[6] = EXP2(SC[14]); ph[7] = EXP2(SC[15]); PIN(ph); SBAR(); \
  } while (0)
  pl = (f32x8){}; ph = (f32x8){};
  PERIOD(sA, sB, kA1, kA2, kB1, kB2, 2, vcur + 672, vcur + TILE_B, true);
  PERIOD(sB, sA, kB1, kB2, kA1, kA2, 3, vcur + TILE_B + 672, vcur + 2 * TILE_B, false);
  gu32* fl = (gu32*)(ws + OFF_FLAGS + (size_t)(LAYER * 256 + head * 8 + w) * 128);
  WAITV(0);
  if (l == 0) __hip_atomic_store(fl + qb, 1u, RLX_AGENT);
  {
    for (unsigned spins = 0; spins < (1u << 20); ++spins) {
      const unsigned f = __hip_atomic_load(fl + (l & 7), RLX_AGENT);
      if (__all(f == 1u)) break;
      __builtin_amdgcn_s_sleep(1);
    }
    __builtin_amdgcn_fence(__ATOMIC_ACQUIRE, "agent");
  }
  const char* rsrc0 = kvh + (size_t)w * TILE_B + l * 16;
  const int pbase = (w < 4) ? 1 : 8;
  auto fetch = [&](int i) {
    const char* g = rsrc0 + (size_t)((qb + i) & 7) * 8 * TILE_B;
    const unsigned d = lds0 + ((pbase + i + 13) % 15) * POS_B + (w & 3) * TILE_B;
    glds16(g, d);
    glds16(g + 1024, d + 1024);
    if (l < 36) glds16(g + 2048, d + 2048);
  };
#pragma unroll 1
  for (int i = 1; i < 7; ++i) fetch(i);
  if (w < 4) fetch(7);
#pragma unroll 1
  for (int it = 0; it < 15; ++it) {
    if (it == 1) { WAIT_BAR(0); if (w >= 4) fetch(7); }
    if (it == 8) WAIT_BAR(0);
    const int so = ((it + 14) % 15) * POS_B;
    vcur = vnext;
    k1p = smem + so + k1o; k2p = smem + so + k2o; vnext = smem + so + vo;
    PERIOD(sA, sB, kA1, kA2, kB1, kB2, 0, vcur + 2 * TILE_B + 672, vcur + 3 * TILE_B, false);
    PERIOD(sB, sA, kB1, kB2, kA1, kA2, 1, vcur + 3 * TILE_B + 672, vnext, false);
    PERIOD(sA, sB, kA1, kA2, kB1, kB2, 2, vnext + 672, vnext + TILE_B, false);
    PERIOD(sB, sA, kB1, kB2, kA1, kA2, 3, vnext + TILE_B + 672, vnext + 2 * TILE_B, false);
  }
  PERIOD(sA, sB, kA1, kA2, kB1, kB2, 0, vnext + 2 * TILE_B + 672, vnext + 3 * TILE_B, false);
  PERIOD(sB, sA, kB1, kB2, kA1, kA2, 1, vnext + 3 * TILE_B + 672, vnext, false);
  o = SMF(v1, ph, o);
  float lsum = h ? o[8] : o[12];
  {
    float chk = lsum;
#pragma unroll
    for (int r = 0; r < 12; ++r) chk = fmaxf(chk, fabsf(o[r]));
    bool bad = !(chk < 1e30f);
#pragma unroll
    for (int r = 0; r < 12; ++r) bad |= (o[r] != o[r]);
    bad |= (lsum != lsum);
    if (__any(bad)) {
      __builtin_amdgcn_fence(__ATOMIC_ACQUIRE, "agent");
      float m = -__builtin_inff();
      o = (f32x16){};
#pragma unroll 1
      for (int t = 0; t < 64; ++t) {
        const char* g = kvh + (size_t)t * TILE_B;
        const bf16x8 k1 = *(const bf16x8*)(g + k1o), w0 = *(const bf16x8*)(g + vo), w1 = *(const bf16x8*)(g + vo + 672);
        const s16x4 k2 = *(const s16x4*)(g + k2o);
        f32x16 s = {};
        s = MFMA32K8(k2, q2, s);
        s = MFMA32(k1, q1, s);
        float mn = m;
#pragma unroll
        for (int r = 0; r < 16; ++r) mn = fmaxf(mn, s[r]);
        mn = fmaxf(mn, __shfl_xor(mn, 32));
        const float al = EXP2(m - mn);
        m = mn;
#pragma unroll
        for (int r = 0; r < 16; ++r) o[r] *= al;
#pragma unroll
        for (int r = 0; r < 8; ++r) { pl[r] = EXP2(s[r] - m); ph[r] = EXP2(s[r + 8] - m); }
        o = SMF(w0, pl, o);
        o = SMF(w1, ph, o);
      }
      lsum = h ? o[8] : o[12];
    }
  }
  const float il = __builtin_amdgcn_rcpf(lsum);
  {
    const int c0 = (5 * head) >> 1, odd = head & 1;
    char* ob = ws + OFF_OB + (size_t)(qb * 8 + w) * 40960 + r32 * 16;
    *(u32x4*)(ob + (c0 + odd + h) * 512) =
        (u32x4){pack2(o[0] * il, o[1] * il), pack2(o[2] * il, o[3] * il), pack2(o[4] * il, o[5] * il), pack2(o[6] * il, o[7] * il)};
    if (!h) *(u32x2*)(ob + (odd ? c0 * 512 + 8 : (c0 + 2) * 512)) = (u32x2){pack2(o[8] * il, o[9] * il), pack2(o[10] * il, o[11] * il)};
  }
}

template <int FINAL>
__global__ __launch_bounds__(256) void k_proj(char* __restrict__ ws, const char* __restrict__ wmat, char* __restrict__ out) {
  extern __shared__ __attribute__((aligned(16))) char smem_[];
  lds_ptr smem = (lds_ptr)smem_;
  const unsigned lds0 = (unsigned)(uintptr_t)smem;
  const int bid = blockIdx.x, tid = threadIdx.x, l = tid & 63, w = __builtin_amdgcn_readfirstlane(tid >> 6);
  const int rb = (bid & 7) * 4 + ((bid >> 3) & 3), cb = bid >> 5;
  const int p = l & 15, g = l >> 4;
  const int s = rb * 64 + w * 16 + p;
  const char* gsrc[5];
#pragma unroll
  for (int i = 0; i < 5; ++i) {
    const int item = 64 * i + l, rho = item / 10, c16 = item - rho * 10;
    const int j = (rho & 19) | ((rho & 4) << 1) | ((rho & 8) >> 1);
    gsrc[i] = wmat + (size_t)(32 * w + j) * 1280 + cb * 160 + c16 * 16;
  }
  const char* op = ws + OFF_OB + (size_t)(s >> 5) * 40960 + g * 512 + (s & 31) * 16;
  u32x4 of[20];
#pragma unroll
  for (int m = 0; m < 5; ++m) {
#pragma unroll
    for (int i = 0; i < 5; ++i) glds16(gsrc[i] + (size_t)m * 128 * 1280, lds0 + ((4 * m + w) * 5 + i) * 1024);
#pragma unroll
    for (int i = 0; i < 4; ++i) GLOAD16(of[4 * m + i], op + (4 * m + (i & 2)) * 2048, (i & 1) * 2048);
  }
  u32x2 res[5];
  const int cq = cb * 20 + g;
#pragma unroll
  for (int T = 0; T < 5; ++T) {
    const int q = cq + 4 * T;
    const char* rp = FINAL ? ws + OFF_X3 + (size_t)q * 16384 + s * 8
                           : ws + OFF_XB + (size_t)(s >> 5) * 40960 + (q >> 1) * 512 + (s & 31) * 16 + (q & 1) * 8;
    asm volatile("global_load_dwordx2 %0, %1, off" : "=v"(res[T]) : "v"(rp) : "memory");
  }
  f32x4 acc[5] = {};
  const lds_ptr ta = smem + (16 * (g >> 1) + 4 * (g & 1) + (p >> 2)) * 160 + (p & 3) * 8;
#pragma unroll
  for (int m = 0; m < 5; ++m) {
    WAIT_BAR(5 + 9 * (4 - m));
#pragma unroll
    for (int i = 0; i < 4; ++i) PIN(of[4 * m + i]);
#pragma unroll
    for (int i = 0; i < 4; ++i) {
      const int ks = 4 * m + i;
      const bf16x8 b = __builtin_bit_cast(bf16x8, of[ks]);
#pragma unroll
      for (int T = 0; T < 5; ++T) {
        const bf16x8 a = cat8(lds_tr(ta + ks * 5120 + T * 32), lds_tr(ta + ks * 5120 + 1280 + T * 32));
        acc[T] = MFMA16(a, b, acc[T]);
      }
    }
  }
  WAITV(0);
#pragma unroll
  for (int T = 0; T < 5; ++T) PIN(res[T]);
  float ssq = 0.f;
#pragma unroll
  for (int T = 0; T < 5; ++T) {
    const int q = cq + 4 * T;
    const float v0 = acc[T][0] + lo16(res[T][0]), v1 = acc[T][1] + hi16(res[T][0]);
    const float v2 = acc[T][2] + lo16(res[T][1]), v3 = acc[T][3] + hi16(res[T][1]);
    const u32x2 ov = {pack2(v0, v1), pack2(v2, v3)};
    if (FINAL) {
      *(u32x2*)(out + (size_t)s * 1280 + q * 8) = ov;
    } else {
      *(u32x2*)(ws + OFF_XA + (size_t)q * 16384 + s * 8) = ov;
      ssq += v0 * v0 + ((q == 0) ? 0.f : v1 * v1) + v2 * v2 + v3 * v3;
    }
  }
  if (!FINAL) {
    ssq += __shfl_xor(ssq, 16);
    ssq += __shfl_xor(ssq, 32);
    if (g == 0) atomicAdd((double*)(ws + OFF_SS2) + s, (double)ssq);
  }
}

extern "C" void kernel_launch(void* const* d_in, const int* in_sizes, int n_in, void* d_out, int out_size, void* d_ws, size_t ws_size,
                              hipStream_t stream) {
  const char* x = (const char*)d_in[0];
  const char* wr = (const char*)d_in[1];
  const char* wi = (const char*)d_in[2];
  const char* wqkv = (const char*)d_in[4];
  const char* wo = (const char*)d_in[5];
  char* ws = (char*)d_ws;
  (void)hipFuncSetAttribute((const void*)k_pre, hipFuncAttributeMaxDynamicSharedMemorySize, 81920);
  (void)hipFuncSetAttribute((const void*)k_attn<0>, hipFuncAttributeMaxDynamicSharedMemorySize, ATT_LDS);
  (void)hipFuncSetAttribute((const void*)k_attn<1>, hipFuncAttributeMaxDynamicSharedMemorySize, ATT_LDS);
  (void)hipFuncSetAttribute((const void*)k_proj<0>, hipFuncAttributeMaxDynamicSharedMemorySize, 102400);
  (void)hipFuncSetAttribute((const void*)k_proj<1>, hipFuncAttributeMaxDynamicSharedMemorySize, 102400);
  k_pre<<<520, 256, 81920, stream>>>(x, wr, wi, wqkv, wo, ws);
  k_col<<<256, 256, 0, stream>>>(ws, 0);
  k_attn<0><<<256, 512, ATT_LDS, stream>>>(ws);
  k_proj<0><<<256, 256, 102400, stream>>>(ws, ws + OFF_WO, (char*)d_out);
  k_col<<<160, 256, 0, stream>>>(ws, 1);
  k_attn<1><<<256, 512, ATT_LDS, stream>>>(ws);
  k_proj<1><<<256, 256, 102400, stream>>>(ws, wo, (char*)d_out);
}
```

```cpp
#include <hip/hip_runtime.h>
#include <stdint.h>

typedef __attribute__((ext_vector_type(2))) __bf16 bf16x2;
typedef __attribute__((ext_vector_type(8))) __bf16 bf16x8;
typedef __attribute__((ext_vector_type(16))) __bf16 bf16x16;
typedef __attribute__((ext_vector_type(4))) short s16x4;
typedef __attribute__((ext_vector_type(2))) float f32x2;
typedef __attribute__((ext_vector_type(4))) float f32x4;
typedef __attribute__((ext_vector_type(8))) float f32x8;
typedef __attribute__((ext_vector_type(16))) float f32x16;
typedef __attribute__((ext_vector_type(2))) unsigned u32x2;
typedef __attribute__((ext_vector_type(4))) unsigned u32x4;
#define LAS __attribute__((address_space(3)))
#define GAS __attribute__((address_space(1)))
typedef LAS char* lds_ptr;
typedef GAS unsigned gu32;

#define SBAR() __builtin_amdgcn_sched_barrier(0)
#define PIN(x) asm volatile("" : "+v"(x))
#define WAITV(N) asm volatile("s_waitcnt vmcnt(%0)" ::"i"(N) : "memory")
#define WAIT_BAR(N) asm volatile("s_waitcnt vmcnt(%0) lgkmcnt(0)\n\ts_barrier" ::"i"(N) : "memory")
#define LBAR() asm volatile("s_waitcnt lgkmcnt(0)\n\ts_barrier" ::: "memory")
#define MFMA32(a, b, c) __builtin_amdgcn_mfma_f32_32x32x16_bf16(a, b, c, 0, 0, 0)
#define MFMA32K8(a, b, c) __builtin_amdgcn_mfma_f32_32x32x8bf16_1k(a, b, c, 0, 0, 0)
#define MFMA16(a, b, c) __builtin_amdgcn_mfma_f32_16x16x32_bf16(a, b, c, 0, 0, 0)
#define RLX_AGENT __ATOMIC_RELAXED, __HIP_MEMORY_SCOPE_AGENT

constexpr size_t SZ_X = 2048ull * 640 * 2;
constexpr size_t OFF_FLAGS = 0;
constexpr size_t OFF_RS = 65536;
constexpr size_t OFF_SS0 = OFF_RS + 16384;
constexpr size_t OFF_SS2 = OFF_SS0 + 16384;
constexpr size_t OFF_XA = OFF_SS2 + 16384;
constexpr size_t OFF_XB = OFF_XA + 2 * SZ_X;
constexpr size_t OFF_OB = OFF_XB + 2 * SZ_X;
constexpr size_t OFF_X3 = OFF_OB + 2 * SZ_X;
constexpr size_t OFF_WF = OFF_X3 + SZ_X;
constexpr size_t OFF_WQ = OFF_WF + SZ_X;
constexpr size_t OFF_FR = OFF_WQ + SZ_X;
constexpr size_t OFF_WO = OFF_FR + 819200;
constexpr size_t OFF_KV = OFF_WO + 819200;
constexpr int TILE_B = 2688;
constexpr size_t SZ_KV = 32ull * 64 * TILE_B;

#define STAMP_DECL
#define STAMP(I)
#define STAMP_END(K)

__device__ __forceinline__ unsigned pack2(float a, float b) {
  f32x2 f = {a, b};
  return __builtin_bit_cast(unsigned, __builtin_convertvector(f, bf16x2));
}
__device__ __forceinline__ float lo16(unsigned u) { return __builtin_bit_cast(float, u << 16); }
__device__ __forceinline__ float hi16(unsigned u) { return __builtin_bit_cast(float, u & 0xffff0000u); }
__device__ __forceinline__ void glds16(const void* g, unsigned lds_base) {
  unsigned sv;
  asm volatile("s_mov_b32 %0, m0\n\ts_mov_b32 m0, %2\n\ts_nop 0\n\tglobal_load_lds_dwordx4 %1, off\n\ts_mov_b32 m0, %0"
               : "=&s"(sv) : "v"(g), "s"(lds_base) : "memory");
}
__device__ __forceinline__ void glds16_sc1(const void* g, unsigned lds_base) {
  unsigned sv;
  asm volatile("s_mov_b32 %0, m0\n\ts_mov_b32 m0, %2\n\ts_nop 0\n\tglobal_load_lds_dwordx4 %1, off sc1\n\ts_mov_b32 m0, %0"
               : "=&s"(sv) : "v"(g), "s"(lds_base) : "memory");
}
template <int SC1>
__device__ __forceinline__ void glds16s(const void* sbase, unsigned voff, unsigned lds_base) {
  unsigned sv;
  if (SC1)
    asm volatile("s_mov_b32 %0, m0\n\ts_mov_b32 m0, %3\n\ts_nop 0\n\tglobal_load_lds_dwordx4 %1, %2 sc1\n\ts_mov_b32 m0, %0"
                 : "=&s"(sv) : "v"(voff), "s"(sbase), "s"(lds_base) : "memory");
  else
    asm volatile("s_mov_b32 %0, m0\n\ts_mov_b32 m0, %3\n\ts_nop 0\n\tglobal_load_lds_dwordx4 %1, %2\n\ts_mov_b32 m0, %0"
                 : "=&s"(sv) : "v"(voff), "s"(sbase), "s"(lds_base) : "memory");
}
__device__ __forceinline__ float dot2(unsigned v, float acc) {
  return __builtin_amdgcn_fdot2_f32_bf16(__builtin_bit_cast(bf16x2, v), __builtin_bit_cast(bf16x2, v), acc, false);
}
#define GLOAD16(dst, ptr, OFF) asm volatile("global_load_dwordx4 %0, %1, off offset:%2" : "=v"(dst) : "v"(ptr), "i"(OFF) : "memory")
__device__ __forceinline__ bf16x8 lds_rd16(lds_ptr p) { return *(const LAS bf16x8*)p; }
__device__ __forceinline__ s16x4 lds_tr(lds_ptr p) {
  return __builtin_amdgcn_ds_read_tr16_b64_v4i16((LAS s16x4*)p);
}
__device__ __forceinline__ bf16x8 cat8(s16x4 a, s16x4 b) {
  typedef __attribute__((ext_vector_type(8))) short s16x8;
  s16x8 r = {a[0], a[1], a[2], a[3], b[0], b[1], b[2], b[3]};
  return __builtin_bit_cast(bf16x8, r);
}

template <bool TR>
__device__ __forceinline__ void k0_gemm(const char* src, int row0, int cg, int kind, char* ws, lds_ptr smem) {
  const int tid = threadIdx.x, l = tid & 63, w = __builtin_amdgcn_readfirstlane(tid >> 6);
  const unsigned lds0 = (unsigned)(uintptr_t)smem;
  STAMP_DECL; STAMP(0);
  {
    const char* g0;
    const char* g1;
    int kstride;
    if (!TR) {
      const int r0 = 16 * w + (l >> 3);
      g0 = src + (size_t)(row0 + r0) * 1280 + (((l & 7) ^ ((l >> 4) & 7)) << 4);
      g1 = src + (size_t)(row0 + r0 + 8) * 1280 + (((l & 7) ^ ((4 + (l >> 4)) & 7)) << 4);
      kstride = 128;
    } else {
      const int n0 = 16 * w + (l >> 3);
      g0 = src + (size_t)n0 * 3840 + row0 * 2 + (((l & 7) ^ (((l >> 4) & 1) << 2)) << 4);
      g1 = g0 + 8 * 3840;
      kstride = 64 * 3840;
    }
#pragma unroll
    for (int kg = 0; kg < 10; ++kg) {
      glds16(g0 + (size_t)kg * kstride, lds0 + (kg * 8 + 2 * w) * 1024);
      glds16(g1 + (size_t)kg * kstride, lds0 + (kg * 8 + 2 * w + 1) * 1024);
    }
  }
  STAMP(1);
  const int h = l >> 5, r32 = l & 31;
  const int cp = cg * 128 + 32 * w + r32;
  float nu = (float)(cp >> 1), amp = 0.05590169943749474f, ph = (cp & 1) ? 0.25f : 0.f;
  if (cp < 2) { amp = 0.03952847075210474f; ph = 0.f; nu = cp ? 0.f : 320.f; }
  float fa[8], fb[8];
#pragma unroll
  for (int j = 0; j < 8; ++j) {
    const float n = (float)(8 * h + j);
    fa[j] = amp * __builtin_amdgcn_cosf(__builtin_fmaf(nu * n, 1.f / 640.f, ph));
    fb[j] = amp * __builtin_amdgcn_cosf(__builtin_fmaf(nu * (n + 16.f), 1.f / 640.f, ph));
  }
  const float co = 2.f * __builtin_amdgcn_cosf(nu * (1.f / 40.f));
  lds_ptr ra[4];
  if (!TR) {
    const int f = (r32 >> 1) & 7;
#pragma unroll
    for (int q = 0; q < 4; ++q) ra[q] = smem + (r32 >> 3) * 1024 + (r32 & 7) * 128 + (((2 * q + h) ^ f) << 4);
  } else {
    const int gm = l >> 4, q = (l >> 2) & 3, p = l & 3;
#pragma unroll
    for (int R = 0; R < 2; ++R)
      ra[R] = smem + (8 * (gm >> 1) + q) * 128 + (((4 * R + 2 * (gm & 1) + (p >> 1)) ^ (((q >> 1) & 1) << 2)) << 4) + (p & 1) * 8;
    ra[2] = ra[3] = ra[0];
  }
  f32x16 acc0 = {}, acc1 = {};
  STAMP(2);
#pragma unroll 1
  for (int st = 0; st < 5; ++st) {
    if (st == 0) WAIT_BAR(16); else if (st == 1) WAIT_BAR(12); else if (st == 2) WAIT_BAR(8); else if (st == 3) WAIT_BAR(4); else WAIT_BAR(0);
    if (st == 0) STAMP(3);
    const int so = st * 16384;
#pragma unroll
    for (int k8 = 0; k8 < 8; ++k8) {
      bf16x8 b0, b1;
      if (!TR) {
        b0 = lds_rd16(ra[k8 & 3] + so + (k8 >> 2) * 8192);
        b1 = lds_rd16(ra[k8 & 3] + so + (k8 >> 2) * 8192 + 4096);
      } else {
        b0 = cat8(lds_tr(ra[0] + so + k8 * 2048), lds_tr(ra[0] + so + k8 * 2048 + 512));
        b1 = cat8(lds_tr(ra[1] + so + k8 * 2048), lds_tr(ra[1] + so + k8 * 2048 + 512));
      }
      float* cur = (k8 & 1) ? fb : fa;
      float* nxt = (k8 & 1) ? fa : fb;
      u32x4 pk = {pack2(cur[0], cur[1]), pack2(cur[2], cur[3]), pack2(cur[4], cur[5]), pack2(cur[6], cur[7])};
      const bf16x8 a = __builtin_bit_cast(bf16x8, pk);
      acc0 = MFMA32(a, b0, acc0);
      acc1 = MFMA32(a, b1, acc1);
#pragma unroll
      for (int j = 0; j < 8; ++j) cur[j] = __builtin_fmaf(co, nxt[j], -cur[j]);
    }
  }
  STAMP(4);
  const int c0 = cg * 128 + 32 * w + 4 * h;
#pragma unroll
  for (int R = 0; R < 2; ++R) {
    f32x16 acc = R ? acc1 : acc0;
    const int row = row0 + 32 * R + r32;
    char* base;
    if (TR) {
      const int which = row / 640, rem = row - which * 640, head = rem / 20, d = rem - head * 20;
      int T, rr;
      if (which == 0) { T = d >> 4; rr = d; }
      else if (which == 1) { T = d >> 4; rr = (d < 16) ? 16 + d : 8 + d; }
      else {
        int e = (head & 1) ? ((d < 4) ? d + 16 : d - 4) : d;
        T = 1;
        rr = (e < 16) ? ((e & 3) | ((e & 4) << 1) | ((e & 8) >> 1)) : e + 4;
      }
      base = ws + OFF_WQ + (size_t)head * 81920 + T * 1024 + rr * 16;
      if (c0 == 0) acc[1] = 0.f;
    } else if (kind == 0) {
      base = ws + OFF_XA + row * 8;
    } else {
      base = ws + OFF_WO + row * 1280;
    }
#pragma unroll
    for (int p = 0; p < 4; ++p) {
      const int c = c0 + 8 * p;
      size_t off;
      if (TR) off = (c >> 4) * 2048 + ((c >> 3) & 1) * 512 + ((c >> 2) & 1) * 8;
      else off = (kind == 0) ? (size_t)(c >> 2) * 16384 : (size_t)c * 2;
      u32x2 v = {pack2(acc[4 * p], acc[4 * p + 1]), pack2(acc[4 * p + 2], acc[4 * p + 3])};
      *(u32x2*)(base + off) = v;
    }
  }
  STAMP(5); WAITV(0); STAMP(6); STAMP_END(0);
}

__global__ __launch_bounds__(256, 2) void k_pre(const char* __restrict__ x, const char* __restrict__ wr, const char* __restrict__ wi,
                                                const char* __restrict__ wqkv, const char* __restrict__ wo, char* __restrict__ ws) {
  extern __shared__ __attribute__((aligned(16))) char smem_[];
  lds_ptr smem = (lds_ptr)smem_;
  const int bid = blockIdx.x, tid = threadIdx.x;
  if (bid < 160) {
    const int i = bid >> 3;
    k0_gemm<false>(x, ((bid & 7) * 4 + i / 5) * 64, i % 5, 0, ws, smem);
  } else if (bid < 288) {
    const int k0 = (bid - 160) * 16;
    const u32x4* s0 = (const u32x4*)(wr + (size_t)k0 * 642);
    const u32x4* s1 = (const u32x4*)(wi + (size_t)k0 * 642);
    u32x4 v0[3], v1[3];
#pragma unroll
    for (int i = 0; i < 3; ++i) {
      const int it = min(tid + 256 * i, 641);
      v0[i] = s0[it];
      v1[i] = s1[it];
    }
#pragma unroll
    for (int i = 0; i < 3; ++i) {
      const int it = min(tid + 256 * i, 641);
      *(LAS u32x4*)(smem + it * 16) = v0[i];
      *(LAS u32x4*)(smem + 10272 + it * 16) = v1[i];
    }
    __syncthreads();
    const LAS unsigned short* L = (const LAS unsigned short*)smem;
#pragma unroll
    for (int rd = 0; rd < 5; ++rd) {
      const int c = rd * 64 + (tid >> 2), i4 = (tid & 3) * 4;
      const int k2 = c ? c : 320;
      u32x4 o;
#pragma unroll
      for (int i = 0; i < 4; ++i) o[i] = (unsigned)L[(i4 + i) * 321 + k2] | ((unsigned)L[5136 + (i4 + i) * 321 + k2] << 16);
      *(u32x4*)(ws + OFF_WF + ((size_t)c * 2048 + k0 + i4) * 4) = o;
    }
  } else if (bid < 320) {
    const int b = bid - 288;
    const int l16 = tid & 15;
    u32x4 v[4][5];
#pragma unroll
    for (int i = 0; i < 4; ++i) {
      const int row = b * 64 + i * 16 + (tid >> 4);
#pragma unroll
      for (int j = 0; j < 5; ++j) v[i][j] = *(const u32x4*)(x + (size_t)row * 1280 + (j * 16 + l16) * 16);
    }
    *(u32x2*)(ws + OFF_FLAGS + (b * 256 + tid) * 8) = (u32x2){0u, 0u};
    if (tid < 64) { *(double*)(ws + OFF_SS2 + (b * 64 + tid) * 8) = 0.0; *(double*)(ws + OFF_RS + (b * 64 + tid) * 8) = 0.0; }
#pragma unroll
    for (int i = 0; i < 4; ++i) {
      float s = 0.f;
#pragma unroll
      for (int j = 0; j < 5; ++j)
#pragma unroll
        for (int e = 0; e < 4; ++e) s += lo16(v[i][j][e]) + hi16(v[i][j][e]);
#pragma unroll
      for (int m = 1; m < 16; m <<= 1) s += __shfl_xor(s, m);
      const float mean = s * (1.f / 640.f);
      float q = 0.f;
#pragma unroll
      for (int j = 0; j < 5; ++j)
#pragma unroll
        for (int e = 0; e < 4; ++e) {
          const float a = lo16(v[i][j][e]) - mean, c = hi16(v[i][j][e]) - mean;
          q += a * a + c * c;
        }
#pragma unroll
      for (int m = 1; m < 16; m <<= 1) q += __shfl_xor(q, m);
      if (l16 == 0) *(double*)(ws + OFF_SS0 + (b * 64 + i * 16 + (tid >> 4)) * 8) = (double)q;
    }
  } else if (bid < 470) {
    const int i = bid - 320;
    k0_gemm<true>(wqkv, (i / 5) * 64, i % 5, 1, ws, smem);
  } else {
    const int i = bid - 470;
    k0_gemm<false>(wo, (i / 5) * 64, i % 5, 2, ws, smem);
  }
}

__device__ __forceinline__ void cs_pair(float rev, unsigned& re, unsigned& im) {
  const float c = __builtin_amdgcn_cosf(rev), s = __builtin_amdgcn_sinf(rev);
  re = pack2(c, s);
  im = pack2(-s, c);
}

__global__ __launch_bounds__(256) void k_col(char* __restrict__ ws, int layer) {
  __shared__ __attribute__((aligned(16))) char smem_[32768];
  lds_ptr smem = (lds_ptr)smem_;
  const int bid = blockIdx.x, tid = threadIdx.x;
  if (bid >= 160) {
    for (int it = (bid - 160) * 256 + tid; it < 51200; it += 96 * 256) {
      const int row = it % 20, ch = (it / 20) % 80, head = it / 1600;
      const int n = head * 20 + row;
      u32x4 o;
#pragma unroll
      for (int i = 0; i < 4; ++i) {
        const int c = ch * 4 + i;
        const float rev = (float)((c * n) % 640) * (1.f / 640.f);
        float a = 0.05590169943749474f * __builtin_amdgcn_cosf(rev), b = -0.05590169943749474f * __builtin_amdgcn_sinf(rev);
        if (c == 0) { b = 0.03952847075210474f; a = (n & 1) ? -b : b; }
        o[i] = pack2(a, b);
      }
      *(u32x4*)(ws + OFF_FR + (size_t)it * 16) = o;
    }
    return;
  }
  const int u = ((bid & 7) * 20 + (bid >> 3));
  const int l = tid & 63, w = __builtin_amdgcn_readfirstlane(tid >> 6), p = l & 15, g = l >> 4;
  STAMP_DECL; STAMP(0);
  const int srow = 32 * (16 * w + 4 * g) + 2 * p;
  u32x4 xr[4];
  double2 ssv[4];
  const char* ssp = ws + (layer ? OFF_SS2 : OFF_SS0);
#pragma unroll
  for (int i = 0; i < 4; ++i) {
    xr[i] = *(const u32x4*)(ws + OFF_XA + layer * SZ_X + (size_t)u * 16384 + (srow + 32 * i) * 8);
    ssv[i] = *(const double2*)(ssp + (srow + 32 * i) * 8);
  }
  unsigned wf[2][8];
#pragma unroll
  for (int c = 0; c < 2; ++c)
#pragma unroll
    for (int i = 0; i < 8; ++i)
      wf[c][i] = *(const unsigned*)(ws + OFF_WF + ((size_t)(2 * u + c) * 2048 + 16 * w + p + 64 * (16 * (i >> 2) + 4 * g + (i & 3))) * 4);
  STAMP(1);
  u32x4 b1r[4], b1i[4], a2r[2][2], a2i[2][2];
  float twr[8], twi[8];
  {
    const float m = (float)(16 * w + p);
#pragma unroll
    for (int k = 0; k < 4; ++k)
#pragma unroll
      for (int i = 0; i < 4; ++i) {
        unsigned re, im;
        cs_pair(m * (float)(16 * k + 4 * g + i) * (1.f / 64.f), re, im);
        b1r[k][i] = re; b1i[k][i] = im;
      }
#pragma unroll
    for (int t2 = 0; t2 < 2; ++t2)
#pragma unroll
      for (int t = 0; t < 2; ++t)
#pragma unroll
        for (int i = 0; i < 4; ++i) {
          unsigned re, im;
          cs_pair((float)(16 * t2 + p) * (float)(16 * t + 4 * g + i) * (1.f / 32.f), re, im);
          a2r[t2][t][i] = re; a2i[t2][t][i] = im;
        }
#pragma unroll
    for (int i = 0; i < 8; ++i) {
      const float rev = m * (float)(16 * (i >> 2) + 4 * g + (i & 3)) * (1.f / 2048.f);
      twr[i] = __builtin_amdgcn_cosf(rev);
      twi[i] = -__builtin_amdgcn_sinf(rev);
    }
  }
  STAMP(2);
  const float mask0 = (u == 0) ? 0.f : 1.f;
  {
    u32x4 z[2][2];
#pragma unroll
    for (int i = 0; i < 4; ++i) {
      const float r0 = __builtin_amdgcn_rsqf(__builtin_fmaf((float)ssv[i].x, 1.f / 640.f, 1e-5f));
      const float r1 = __builtin_amdgcn_rsqf(__builtin_fmaf((float)ssv[i].y, 1.f / 640.f, 1e-5f));
      z[0][0][i] = pack2(lo16(xr[i][0]) * r0, hi16(xr[i][0]) * (r0 * mask0));
      z[1][0][i] = pack2(lo16(xr[i][1]) * r0, hi16(xr[i][1]) * r0);
      z[0][1][i] = pack2(lo16(xr[i][2]) * r1, hi16(xr[i][2]) * (r1 * mask0));
      z[1][1][i] = pack2(lo16(xr[i][3]) * r1, hi16(xr[i][3]) * r1);
    }
#pragma unroll
    for (int c = 0; c < 2; ++c)
#pragma unroll
      for (int e = 0; e < 2; ++e) {
        const int n2 = 2 * p + e;
        *(LAS u32x4*)(smem + c * 8192 + n2 * 256 + (((4 * w + g) ^ (n2 & 15)) << 4)) = z[c][e];
      }
  }
  STAMP(3);
  __syncthreads();
  STAMP(4);
#pragma unroll
  for (int c = 0; c < 2; ++c) {
    f32x4 tr[2] = {}, ti[2] = {};
#pragma unroll
    for (int t = 0; t < 2; ++t)
#pragma unroll
      for (int k = 0; k < 4; ++k) {
        const bf16x8 a = lds_rd16(smem + c * 8192 + (16 * t + p) * 256 + (((4 * k + g) ^ p) << 4));
        tr[t] = MFMA16(a, __builtin_bit_cast(bf16x8, b1r[k]), tr[t]);
        ti[t] = MFMA16(a, __builtin_bit_cast(bf16x8, b1i[k]), ti[t]);
      }
    u32x4 bb[2];
#pragma unroll
    for (int i = 0; i < 8; ++i) {
      const float a = tr[i >> 2][i & 3], b = ti[i >> 2][i & 3];
      bb[i >> 2][i & 3] = pack2(a * twr[i] - b * twi[i], a * twi[i] + b * twr[i]);
    }
    f32x4 xr2[2] = {}, xi2[2] = {};
#pragma unroll
    for (int t2 = 0; t2 < 2; ++t2)
#pragma unroll
      for (int t = 0; t < 2; ++t) {
        xr2[t2] = MFMA16(__builtin_bit_cast(bf16x8, a2r[t2][t]), __builtin_bit_cast(bf16x8, bb[t]), xr2[t2]);
        xi2[t2] = MFMA16(__builtin_bit_cast(bf16x8, a2i[t2][t]), __builtin_bit_cast(bf16x8, bb[t]), xi2[t2]);
      }
#pragma unroll
    for (int i = 0; i < 8; ++i) {
      const float a = xr2[i >> 2][i & 3], b = xi2[i >> 2][i & 3], fr = lo16(wf[c][i]), fi = hi16(wf[c][i]);
      bb[i >> 2][i & 3] = pack2(a * fr - b * fi, -(a * fi + b * fr));
    }
    f32x4 ur[2] = {}, ui[2] = {};
#pragma unroll
    for (int t2 = 0; t2 < 2; ++t2)
#pragma unroll
      for (int t = 0; t < 2; ++t) {
        ur[t2] = MFMA16(__builtin_bit_cast(bf16x8, a2r[t2][t]), __builtin_bit_cast(bf16x8, bb[t]), ur[t2]);
        ui[t2] = MFMA16(__builtin_bit_cast(bf16x8, a2i[t2][t]), __builtin_bit_cast(bf16x8, bb[t]), ui[t2]);
      }
#pragma unroll
    for (int i = 0; i < 8; ++i) {
      const float a = ur[i >> 2][i & 3], b = ui[i >> 2][i & 3];
      const int n2 = 16 * (i >> 2) + 4 * g + (i & 3);
      const int rw = (n2 >> 1) + 16 * (n2 & 1);
      *(LAS unsigned*)(smem + 16384 + c * 8192 + rw * 256 + (((4 * w + (p >> 2)) ^ (rw & 15)) << 4) + (p & 3) * 4) =
          pack2(a * twr[i] - b * twi[i], a * twi[i] + b * twr[i]);
    }
  }
  STAMP(5);
  __syncthreads();
  STAMP(6);
  f32x4 yr[2][2] = {}, yi[2][2] = {};
#pragma unroll
  for (int c = 0; c < 2; ++c)
#pragma unroll
    for (int t = 0; t < 2; ++t)
#pragma unroll
      for (int k = 0; k < 4; ++k) {
        const bf16x8 b = lds_rd16(smem + 16384 + c * 8192 + (16 * t + p) * 256 + (((4 * k + g) ^ p) << 4));
        yr[c][t] = MFMA16(__builtin_bit_cast(bf16x8, b1r[k]), b, yr[c][t]);
        yi[c][t] = MFMA16(__builtin_bit_cast(bf16x8, b1i[k]), b, yi[c][t]);
      }
  const float sc = 1.f / 2048.f, sc0 = -sc * mask0;
  char* ob = ws + OFF_XB + layer * SZ_X + (size_t)(16 * w + 4 * g) * 40960 + (u >> 1) * 512 + (2 * p) * 16 + (u & 1) * 8;
#pragma unroll
  for (int r = 0; r < 4; ++r)
#pragma unroll
    for (int t = 0; t < 2; ++t) {
      const unsigned x0 = xr[r][2 * t], x1 = xr[r][2 * t + 1];
      u32x2 o = {pack2(__builtin_fmaf(yr[0][t][r], sc, lo16(x0)), __builtin_fmaf(yi[0][t][r], sc0, hi16(x0))),
                 pack2(__builtin_fmaf(yr[1][t][r], sc, lo16(x1)), __builtin_fmaf(yi[1][t][r], -sc, hi16(x1)))};
      *(u32x2*)(ob + (size_t)r * 40960 + t * 16) = o;
    }
  STAMP(7); WAITV(0); STAMP(8); STAMP_END(layer ? 4 : 1);
}

constexpr int POS_B = 4 * TILE_B;
constexpr int ATT_LDS = 163840;
__host__ __device__ constexpr int slot_off(int pos) { return ((pos + 11) % 15) * POS_B; }
#define EXP2(x) __builtin_amdgcn_exp2f(x)
#define MX3(a, b, c) __builtin_fmaxf(__builtin_fmaxf((a), (b)), (c))
#define SMF(a, b, c) __builtin_amdgcn_smfmac_f32_32x32x32_bf16(a, __builtin_bit_cast(bf16x16, b), c, sidx, 0, 0)
#define A_QB(S, K, Q) "v_mfma_f32_32x32x16_bf16 " S ", " K ", " Q ", 0\n\t"
#define A_QA(S, K, Q) "v_mfma_f32_32x32x8_bf16 " S ", " K ", " Q ", " S "\n\t"
#define A_PV(O, V, P) "v_smfmac_f32_32x32x32_bf16 " O ", " V ", " P ", v112\n\t"
#define A_E4(d0, d1, d2, d3, s0, s1, s2, s3) \
  "v_exp_f32 v" #d0 ", v" #s0 "\n\tv_exp_f32 v" #d1 ", v" #s1 "\n\tv_exp_f32 v" #d2 ", v" #s2 "\n\tv_exp_f32 v" #d3 ", v" #s3 "\n\t"
#define A_PAIR(KC1, KC2, KN1, KN2, KOFF, V1RD, V0RD)                                          \
  A_QB("v[48:63]", KC1, "v[84:87]") A_E4(64, 65, 66, 67, 32, 33, 34, 35)                      \
  A_QA("v[48:63]", KC2, "v[90:91]")                                                           \
  "ds_read_b128 " KC1 ", v113 offset:" KOFF "\n\tds_read_b64 " KC2 ", v114 offset:" KOFF "\n\t" \
  A_E4(68, 69, 70, 71, 36, 37, 38, 39)                                                        \
  A_PV("v[16:31]", "v[108:111]", "v[72:79]") "ds_read_b128 v[108:111], " V1RD "\n\t"          \
  A_E4(72, 73, 74, 75, 40, 41, 42, 43)                                                        \
  "s_waitcnt lgkmcnt(3)\n\t" A_PV("v[0:15]", "v[104:107]", "v[64:71]") A_E4(76, 77, 78, 79, 44, 45, 46, 47) \
  A_QB("v[32:47]", KN1, "v[80:83]") A_E4(64, 65, 66, 67, 48, 49, 50, 51)                      \
  A_QA("v[32:47]", KN2, "v[88:89]") A_E4(68, 69, 70, 71, 52, 53, 54, 55)                      \
  "s_waitcnt lgkmcnt(0)\n\t" A_PV("v[0:15]", "v[108:111]", "v[72:79]") A_E4(72, 73, 74, 75, 56, 57, 58, 59) \
  A_PV("v[16:31]", "v[104:107]", "v[64:71]") "ds_read_b128 v[104:107], " V0RD "\n\t"          \
  A_E4(76, 77, 78, 79, 60, 61, 62, 63)

template <int LAYER>
__global__ __launch_bounds__(512, 2) void k_attn(char* __restrict__ ws) {
  extern __shared__ __attribute__((aligned(16))) char smem_[];
  lds_ptr smem = (lds_ptr)smem_;
  const unsigned lds0 = (unsigned)(uintptr_t)smem;
  const int bid = blockIdx.x, tid = threadIdx.x, l = tid & 63, w = __builtin_amdgcn_readfirstlane(tid >> 6);
  const int h = l >> 5, r32 = l & 31;
  const int head = ((bid & 7) >> 1) * 8 + ((bid >> 3) & 7), qb = (bid & 1) * 4 + (bid >> 6);
  const bool early = w < 4;
  STAMP_DECL; STAMP(0);
  const char* xp = ws + OFF_XB + LAYER * SZ_X + (size_t)(qb * 8 + w) * 40960 + l * 16;
#define XUP 8
#define XRAMP 8
#define XSLOT 16
  u32x4 xf[XSLOT];
  LAS unsigned* cnts = (LAS unsigned*)(smem + 15 * POS_B + 2048);
  gu32* rsg = (gu32*)(ws + OFF_RS) + LAYER * 2048 + qb * 256;
  if (!early) {
    const int v = w - 4;
    const char* g = ws + OFF_WQ + (size_t)head * 81920 + v * 1024;
    const char* f = ws + OFF_FR + (size_t)head * 25600;
    const unsigned lo = l * 16;
    u32x4 sv[3];
    if (l < 8) cnts[l] = 0u;
    auto stage = [&](int s) {
#pragma unroll
      for (int i = 0; i < 4; ++i) glds16s<0>(g + (s * 4 + i) * 4096, lo, lds0 + (s * 4 + i) * 4096 + v * 1024);
      if (LAYER) {
        glds16s<0>(f + (s * 5 + v) * 1024, lo, lds0 + 81920 + (s * 5 + v) * 1024);
        if (l < 16) glds16s<0>(f + (s * 5 + 4) * 1024 + v * 256, lo, lds0 + 81920 + (s * 5 + 4) * 1024 + v * 256);
      }
    };
    constexpr int PS = LAYER ? 6 : 4;
    stage(0);
    {
      const char* sp = ws + OFF_XB + LAYER * SZ_X + (size_t)(qb * 8 + (head >> 2)) * 40960 + ((head & 3) * 8 + 2 * v + (l & 1)) * 16 + (l >> 1) * 512;
      GLOAD16(sv[0], sp, 0);
      GLOAD16(sv[1], sp + 16384, 0);
      GLOAD16(sv[2], sp + 32768 - h * 8192, 0);
    }
    stage(1);
    constexpr int TN = 0;
    WAIT_BAR(PS + 3 + TN);
    stage(2);
    WAITV(2 * PS + TN);
    PIN(sv[0]); PIN(sv[1]); PIN(sv[2]);
    if (l < 2) sv[0][0] &= 0xffffu;
    float q = 0.f;
#pragma unroll
    for (int e = 0; e < 4; ++e) q = dot2(sv[2][e], q);
    if (h) q = 0.f;
#pragma unroll
    for (int e = 0; e < 4; ++e) q = dot2(sv[1][e], dot2(sv[0][e], q));
#pragma unroll
    for (int m = 2; m < 64; m <<= 1) q += __shfl_xor(q, m);
    const float r = __builtin_amdgcn_rsqf(__builtin_fmaf(q, 1.f / 640.f, 1e-5f));
    if (l < 2) __hip_atomic_store(rsg + head * 8 + 2 * v + l, __builtin_bit_cast(unsigned, -r), RLX_AGENT);
    WAIT_BAR(PS + 1 + TN);
    stage(3);
    stage(4);
    WAIT_BAR(2 * PS);
    WAIT_BAR(0);
  }
#pragma unroll
  for (int i = 0; i < XUP; ++i) GLOAD16(xf[i], xp + (i >> 2) * 4096, (i & 3) * 1024);
  asm volatile("s_barrier" ::: "memory");
  STAMP(1);
  f32x16 t0 = {}, t1 = {}, t2 = {};
  unsigned rsu;
  {
    const lds_ptr wa = smem + l * 16;
    const lds_ptr fa = smem + 81920 + (h * 20 + (r32 < 20 ? r32 : (r32 < 28 ? 0 : 16))) * 16;
    bf16x8 a0[4], a1[4], a2[4];
#pragma unroll
    for (int i = 0; i < 3; ++i) {
      a0[i] = lds_rd16(wa + i * 2048); a1[i] = lds_rd16(wa + i * 2048 + 1024);
      if (LAYER) a2[i] = lds_rd16(fa + i * 640);
    }
#pragma unroll
    for (int ks = 0; ks < 40; ++ks) {
      if (ks + 3 < 40) {
        if (((ks + 3) & 7) == 0 && early) asm volatile("s_barrier" ::: "memory");
        a0[(ks + 3) & 3] = lds_rd16(wa + (ks + 3) * 2048);
        a1[(ks + 3) & 3] = lds_rd16(wa + (ks + 3) * 2048 + 1024);
        if (LAYER) a2[(ks + 3) & 3] = lds_rd16(fa + (ks + 3) * 640);
      }
      const int is0 = ks < XRAMP ? XUP + 2 * ks : XUP + XRAMP + ks, issued = is0 < 40 ? is0 : 40;
      WAITV(issued - ks - 1 + (ks > 30 ? 1 : 0));
      PIN(xf[ks % XSLOT]);
      SBAR();
      const bf16x8 b = __builtin_bit_cast(bf16x8, xf[ks % XSLOT]);
      t0 = MFMA32(a0[ks & 3], b, t0);
      t1 = MFMA32(a1[ks & 3], b, t1);
      if (LAYER) t2 = MFMA32(a2[ks & 3], b, t2);
      SBAR();
      if (is0 < 40) GLOAD16(xf[is0 % XSLOT], xp + (is0 >> 2) * 4096, (is0 & 3) * 1024);
      if (ks < XRAMP && is0 + 1 < 40) GLOAD16(xf[(is0 + 1) % XSLOT], xp + ((is0 + 1) >> 2) * 4096, ((is0 + 1) & 3) * 1024);
      if (ks == 30) asm volatile("global_load_dword %0, %1, off sc1" : "=v"(rsu) : "v"(rsg + w * 32 + r32) : "memory");
    }
  }
  STAMP(2);
  asm volatile("s_waitcnt vmcnt(0)" : "+v"(rsu)::"memory");
  for (unsigned spins = 0; spins < (1u << 20) && __any(rsu == 0u); ++spins) {
    __builtin_amdgcn_s_sleep(1);
    rsu = __hip_atomic_load(rsg + w * 32 + r32, RLX_AGENT);
  }
  const float rs = -__builtin_bit_cast(float, rsu);
  const float qs = rs * (0.22360679774997896f * 1.4426950408889634f);
  const float s2 = h ? rs : qs;
  const u32x2 e2 = {pack2(t1[8] * s2, t1[9] * s2), pack2(t1[10] * s2, t1[11] * s2)};
  const u32x2 k2u = {pack2(t1[12] * rs, t1[13] * rs), pack2(t1[14] * rs, t1[15] * rs)};
  const u32x4 k1u = {pack2(t0[8] * rs, t0[9] * rs), pack2(t0[10] * rs, t0[11] * rs), pack2(t0[12] * rs, t0[13] * rs), pack2(t0[14] * rs, t0[15] * rs)};
  u32x4 qa1 = {pack2(t0[0] * qs, t0[1] * qs), pack2(t0[2] * qs, t0[3] * qs), pack2(t0[4] * qs, t0[5] * qs), pack2(t0[6] * qs, t0[7] * qs)};
  u32x2 qa2 = {h ? 0u : e2[0], h ? 0u : e2[1]};
  const lds_ptr mine = smem + slot_off(0) + w * TILE_B;
  {
    const u32x2 k2z = {h ? 0u : k2u[0], h ? 0u : k2u[1]};
    f32x16 s = {};
    s = MFMA32(__builtin_bit_cast(bf16x8, k1u), __builtin_bit_cast(bf16x8, qa1), s);
    s = MFMA32K8(__builtin_bit_cast(s16x4, k2z), __builtin_bit_cast(s16x4, qa2), s);
    *(LAS u32x4*)(mine + l * 16) = k1u;
    *(LAS u32x4*)(smem + 10 * POS_B + w * 1024 + l * 16) = qa1;
    const lds_ptr vb = mine + 1280 + (r32 >> 3) * 352 + (r32 & 7) * 2;
#pragma unroll
    for (int r = 0; r < 8; r += 2) {
      const unsigned pv = pack2(t1[r] * rs, t1[r + 1] * rs);
      const int sg = (r & 3) + 8 * (r >> 2);
      *(LAS unsigned short*)(vb + (sg + 4 * h) * 16) = (unsigned short)pv;
      *(LAS unsigned short*)(vb + (sg + 1 + 4 * h) * 16) = (unsigned short)(pv >> 16);
    }
    const unsigned pa = h ? e2[0] : 0x00003f80u;
    *(LAS unsigned short*)(vb + (20 - 4 * h) * 16) = (unsigned short)pa;
    *(LAS unsigned short*)(vb + (21 - 4 * h) * 16) = (unsigned short)(pa >> 16);
    if (h) {
      *(LAS unsigned short*)(vb + 18 * 16) = (unsigned short)e2[1];
      *(LAS unsigned short*)(vb + 19 * 16) = (unsigned short)(e2[1] >> 16);
    } else {
      *(LAS u32x2*)(mine + 1024 + r32 * 8) = k2u;
    }
    float m0 = MX3(s[0], s[1], s[2]), m1 = MX3(s[3], s[4], s[5]);
    m0 = MX3(m0, s[6], s[7]); m1 = MX3(m1, s[8], s[9]);
    m0 = MX3(m0, s[10], s[11]); m1 = MX3(m1, s[12], s[13]);
    m0 = MX3(m0, s[14], s[15]);
    m0 = fmaxf(m0, m1);
    const auto sw = __builtin_amdgcn_permlane32_swap(__builtin_bit_cast(unsigned, m0), __builtin_bit_cast(unsigned, m0), false, false);
    const float m = fmaxf(__builtin_bit_cast(float, sw[0]), __builtin_bit_cast(float, sw[1]));
    const float mh = hi16(pack2(0.f, -m));
    if (h) qa2[0] = pack2(mh, -m - mh);
    *(LAS u32x2*)(smem + (early ? 15 * POS_B + w * 512 : 10 * POS_B + 6144 + w * 512) + l * 8) = qa2;
  }
  char* kvh = ws + (unsigned)__builtin_amdgcn_readfirstlane((unsigned)(OFF_KV + LAYER * SZ_KV) + head * 64 * TILE_B);
  const int k1o = l * 16, k2o = h ? 1280 + 20 * 16 : 1024 + r32 * 8, vo = 1280 + h * 352 + (r32 < 21 ? r32 : 21) * 16;
  {
    const __amdgpu_buffer_rsrc_t rsrc = __builtin_amdgcn_make_buffer_rsrc(kvh + (unsigned)((qb * 8 + w) * TILE_B), 0, TILE_B, 0x00020000);
    __builtin_amdgcn_raw_buffer_store_b128(k1u, rsrc, l * 16, 0, 16);
    asm volatile("" ::: "memory");
    const u32x4 v1 = *(const LAS u32x4*)(mine + 1024 + l * 16);
    const u32x4 v2 = *(const LAS u32x4*)(mine + min(2048 + l * 16, TILE_B - 16));
    __builtin_amdgcn_raw_buffer_store_b128(v1, rsrc, 1024 + l * 16, 0, 16);
    __builtin_amdgcn_raw_buffer_store_b128(v2, rsrc, 2048 + l * 16, 0, 16);
  }
  if (LAYER) {
    char* x3 = ws + OFF_X3 + (size_t)(5 * head + h) * 16384 + (qb * 256 + w * 32 + r32) * 8;
    *(u32x2*)(x3) = (u32x2){pack2(t2[0], t2[1]), pack2(t2[2], t2[3])};
    *(u32x2*)(x3 + 2 * 16384) = (u32x2){pack2(t2[4], t2[5]), pack2(t2[6], t2[7])};
    if (!h) *(u32x2*)(x3 + 4 * 16384) = (u32x2){pack2(t2[8], t2[9]), pack2(t2[10], t2[11])};
  }
  gu32* fl = (gu32*)(ws + OFF_FLAGS + (size_t)(LAYER * 256 + head * 8 + w) * 128);
  auto publish = [&]() {
    WAITV(0);
    if (l == 0) __hip_atomic_store(fl + qb, 1u, RLX_AGENT);
  };
  auto poll = [&]() {
    for (unsigned spins = 0; spins < (1u << 20); ++spins) {
      const unsigned f = __hip_atomic_load(fl + (l & 7), RLX_AGENT);
      if (__all(f == 1u)) break;
      __builtin_amdgcn_s_sleep(2);
    }
    asm volatile("" ::: "memory");
  };
  auto fetch = [&](int i0, int i1) {
#pragma unroll 1
    for (int i = i0; i < i1; ++i) {
      const char* g = kvh + (unsigned)__builtin_amdgcn_readfirstlane((((qb + i) & 7) * 8 + w) * TILE_B);
      const int ps = (early ? 12 : 4) + i;
      const unsigned d = lds0 + (ps >= 15 ? ps - 15 : ps) * POS_B + (w & 3) * TILE_B;
      unsigned sv;
      asm volatile("s_mov_b32 %0, m0\n\ts_mov_b32 m0, %3\n\ts_nop 0\n\t"
                   "global_load_lds_dwordx4 %1, %2 sc1\n\tglobal_load_lds_dwordx4 %1, %2 offset:1024 sc1\n\t"
                   "s_mov_b32 exec_hi, 0xff\n\tglobal_load_lds_dwordx4 %1, %2 offset:2048 sc1\n\ts_mov_b32 exec_hi, -1\n\ts_mov_b32 m0, %0"
                   : "=&s"(sv) : "v"(k1o), "s"(g), "s"(d) : "memory");
    }
  };
  auto signal = [&](int i) { if (l == 0) __hip_atomic_fetch_add(cnts + i, 1u, __ATOMIC_RELAXED, __HIP_MEMORY_SCOPE_WORKGROUP); };
  auto await = [&](int i, unsigned n) {
    while (__builtin_amdgcn_readfirstlane(__hip_atomic_load(cnts + i, __ATOMIC_RELAXED, __HIP_MEMORY_SCOPE_WORKGROUP)) < n) __builtin_amdgcn_s_sleep(2);
    asm volatile("" ::: "memory");
  };
  unsigned sidx = 0xDDDDDDDDu;
  u32x4 v1 = {};
  f32x16 oA, oB, sB, pp;
  asm volatile("s_nop 1\n\tv_mfma_f32_32x32x8_bf16 v[0:15], v[108:109], v[108:109], 0\n\tv_mfma_f32_32x32x8_bf16 v[16:31], v[108:109], v[108:109], 0\n\t"
               "v_mfma_f32_32x32x8_bf16 v[64:79], v[108:109], v[108:109], 0"
               : "={v[0:15]}"(oA), "={v[16:31]}"(oB), "={v[48:63]}"(sB), "={v[64:79]}"(pp), "+{v[108:111]}"(v1));
  STAMP(3);
  if (early) { publish(); poll(); fetch(1, 3); WAITV(0); }
  STAMP(4);
  LBAR();
  STAMP(5);
  const int kof = (w >> 2) * 2 * TILE_B + (int)lds0;
  const int bk1 = kof + k1o, bk2 = kof + k2o, bv = kof + vo;
  u32x4 qb1 = *(const LAS u32x4*)(smem + 10 * POS_B + (w ^ 4) * 1024 + l * 16);
  u32x2 qb2 = *(const LAS u32x2*)(smem + (early ? 10 * POS_B + 6144 + (w ^ 4) * 512 : 15 * POS_B + (w ^ 4) * 512) + l * 8);
  const lds_ptr p0 = smem + slot_off(0) + (w >> 2) * 2 * TILE_B;
  u32x4 kA1 = *(const LAS u32x4*)(p0 + k1o), kB1 = *(const LAS u32x4*)(p0 + TILE_B + k1o), v0 = *(const LAS u32x4*)(p0 + vo);
  u32x2 kA2 = *(const LAS u32x2*)(p0 + k2o), kB2 = *(const LAS u32x2*)(p0 + TILE_B + k2o);
  f32x16 sA = {};
  sA = MFMA32(__builtin_bit_cast(bf16x8, kA1), __builtin_bit_cast(bf16x8, qa1), sA);
  sA = MFMA32K8(__builtin_bit_cast(s16x4, kA2), __builtin_bit_cast(s16x4, qa2), sA);
  if (early) fetch(3, 8);
  u32x4 adr = {0u, 0u, 0u, (unsigned)(bv + slot_off(0))};
  unsigned so = slot_off(0);
  asm volatile("s_nop 7\n\ts_nop 7" : "+v"(sA));
  if (!early) __builtin_amdgcn_s_setprio(1);
#pragma unroll 1
  for (int seg = 0; seg < 7; ++seg) {
    int cnt = (0x6232111 >> (4 * seg)) & 15;
    if (seg == 0) STAMP(6);
    if (seg == 1) {
      STAMP(7);
      signal(2);
      if (!early) publish();
      STAMP(8);
    } else if (seg == 2) {
      if (early) { WAITV(0); signal(1); }
    } else if (seg == 3) {
      STAMP(9);
      if (!early) { poll(); await(2, 8); fetch(1, 8); }
      STAMP(10);
      await(1, 4);
      STAMP(11);
    } else if (seg == 4) {
      if (!early) { WAITV(0); signal(3); }
    } else if (seg == 5) {
      STAMP(12);
      signal(4);
      await(3, 4);
      STAMP(13);
    } else if (seg == 6) {
      __builtin_amdgcn_s_setprio(0);
    }
    asm volatile(
        "1:\n\t"
        "s_add_u32 %[so], %[so], %[pos]\n\t"
        "s_cmp_eq_u32 %[so], %[lim]\n\t"
        "s_cselect_b32 %[so], 0, %[so]\n\t"
        "v_mov_b32 v115, v116\n\t"
        "v_add_u32 v113, %[so], %[bk1]\n\t"
        "v_add_u32 v114, %[so], %[bk2]\n\t"
        "v_add_u32 v116, %[so], %[bv]\n\t"
        A_PAIR("v[92:95]", "v[100:101]", "v[96:99]", "v[102:103]", "0", "v115 offset:704", "v115 offset:2688")
        A_PAIR("v[96:99]", "v[102:103]", "v[92:95]", "v[100:101]", "2688", "v115 offset:3392", "v116")
        "s_sub_u32 %[cnt], %[cnt], 1\n\t"
        "s_cmp_lg_u32 %[cnt], 0\n\t"
        "s_cbranch_scc1 1b\n\t"
        "s_waitcnt lgkmcnt(0)"
        : "+{v[0:15]}"(oA), "+{v[16:31]}"(oB), "+{v[32:47]}"(sA), "+{v[48:63]}"(sB), "+{v[64:79]}"(pp), "+{v[80:83]}"(qa1),
          "+{v[84:87]}"(qb1), "+{v[88:89]}"(qa2), "+{v[90:91]}"(qb2), "+{v[92:95]}"(kA1), "+{v[96:99]}"(kB1), "+{v[100:101]}"(kA2),
          "+{v[102:103]}"(kB2), "+{v[104:107]}"(v0), "+{v[108:111]}"(v1), "+{v112}"(sidx), "+{v113}"(adr[0]), "+{v114}"(adr[1]),
          "+{v115}"(adr[2]), "+{v116}"(adr[3]), [so] "+s"(so), [cnt] "+s"(cnt)
        : [bk1] "v"(bk1), [bk2] "v"(bk2), [bv] "v"(bv), [pos] "i"(POS_B), [lim] "i"(15 * POS_B)
        : "scc", "memory");
  }
  asm volatile("s_nop 1\n\t" A_PV("v[16:31]", "v[108:111]", "v[72:79]") "s_nop 7\n\ts_nop 7"
               : "+{v[0:15]}"(oA), "+{v[16:31]}"(oB), "+{v[64:79]}"(pp), "+{v[108:111]}"(v1), "+{v112}"(sidx));
  STAMP(14);
  {
    await(4, 8);
    const lds_ptr ex = smem + w * 3072 + l * 16;
#pragma unroll
    for (int i = 0; i < 3; ++i) *(LAS f32x4*)(ex + i * 1024) = (f32x4){oB[4 * i], oB[4 * i + 1], oB[4 * i + 2], oB[4 * i + 3]};
    LBAR();
    const lds_ptr ey = smem + (w ^ 4) * 3072 + l * 16;
#pragma unroll
    for (int i = 0; i < 3; ++i) {
      const f32x4 v = *(const LAS f32x4*)(ey + i * 1024);
#pragma unroll
      for (int j = 0; j < 4; ++j) oA[4 * i + j] += v[j];
    }
  }
  float lsum = __shfl(oA[8], r32 + 32);
  {
    float chk = lsum;
#pragma unroll
    for (int r = 0; r < 12; ++r) chk = fmaxf(chk, fabsf(oA[r]));
    bool bad = !(chk < 1e30f) || !(lsum > 1e-20f);
#pragma unroll
    for (int r = 0; r < 12; ++r) bad |= (oA[r] != oA[r]);
    if (__any(bad)) {
      __builtin_amdgcn_fence(__ATOMIC_ACQUIRE, "agent");
      float m = -__builtin_inff();
      oA = (f32x16){};
      const u32x2 q2z = {h ? 0u : qa2[0], qa2[1]};
#pragma unroll 1
      for (int t = 0; t < 64; ++t) {
        const char* g = kvh + (size_t)t * TILE_B;
        const bf16x8 k1 = *(const bf16x8*)(g + k1o), w0 = *(const bf16x8*)(g + vo), w1 = *(const bf16x8*)(g + vo + 704);
        const s16x4 k2 = *(const s16x4*)(g + k2o);
        f32x16 s = {};
        s = MFMA32K8(k2, __builtin_bit_cast(s16x4, q2z), s);
        s = MFMA32(k1, __builtin_bit_cast(bf16x8, qa1), s);
        float mn = m;
#pragma unroll
        for (int r = 0; r < 16; ++r) mn = fmaxf(mn, s[r]);
        mn = fmaxf(mn, __shfl_xor(mn, 32));
        const float al = EXP2(m - mn);
        m = mn;
        f32x8 pl, ph;
#pragma unroll
        for (int r = 0; r < 16; ++r) oA[r] *= al;
#pragma unroll
        for (int r = 0; r < 8; ++r) { pl[r] = EXP2(s[r] - m); ph[r] = EXP2(s[r + 8] - m); }
        oA = SMF(w0, pl, oA);
        oA = SMF(w1, ph, oA);
      }
      lsum = __shfl(oA[8], r32 + 32);
    }
  }
  const float il = __builtin_amdgcn_rcpf(lsum);
  {
    const int c0 = (5 * head) >> 1, odd = head & 1;
    char* ob = ws + OFF_OB + LAYER * SZ_X + (size_t)(qb * 8 + w) * 40960 + r32 * 16;
    *(u32x4*)(ob + (c0 + odd + h) * 512) =
        (u32x4){pack2(oA[0] * il, oA[1] * il), pack2(oA[2] * il, oA[3] * il), pack2(oA[4] * il, oA[5] * il), pack2(oA[6] * il, oA[7] * il)};
    if (!h) *(u32x2*)(ob + (odd ? c0 * 512 + 8 : (c0 + 2) * 512)) = (u32x2){pack2(oA[8] * il, oA[9] * il), pack2(oA[10] * il, oA[11] * il)};
  }
  STAMP(15); STAMP_END(LAYER ? 5 : 2);
}

template <int FINAL>
__global__ __launch_bounds__(256) void k_proj(char* __restrict__ ws, const char* __restrict__ wmat, char* __restrict__ out) {
  extern __shared__ __attribute__((aligned(16))) char smem_[];
  lds_ptr smem = (lds_ptr)smem_;
  const unsigned lds0 = (unsigned)(uintptr_t)smem;
  const int bid = blockIdx.x, tid = threadIdx.x, l = tid & 63, w = __builtin_amdgcn_readfirstlane(tid >> 6);
  const int rb = (bid & 7) * 4 + ((bid >> 3) & 3), cb = bid >> 5;
  const int p = l & 15, g = l >> 4;
  const int s = rb * 64 + w * 16 + p;
  STAMP_DECL; STAMP(0);
  const char* gsrc[5];
#pragma unroll
  for (int i = 0; i < 5; ++i) {
    const int item = 64 * i + l, rho = item / 10, c16 = item - rho * 10;
    const int j = (rho & 19) | ((rho & 4) << 1) | ((rho & 8) >> 1);
    gsrc[i] = wmat + (size_t)(32 * w + j) * 1280 + cb * 160 + c16 * 16;
  }
  const char* op = ws + OFF_OB + FINAL * SZ_X + (size_t)(s >> 5) * 40960 + g * 512 + (s & 31) * 16;
  u32x4 of[20];
#pragma unroll
  for (int m = 0; m < 5; ++m) {
#pragma unroll
    for (int i = 0; i < 5; ++i) glds16(gsrc[i] + (size_t)m * 128 * 1280, lds0 + ((4 * m + w) * 5 + i) * 1024);
#pragma unroll
    for (int i = 0; i < 4; ++i) GLOAD16(of[4 * m + i], op + (4 * m + (i & 2)) * 2048, (i & 1) * 2048);
  }
  u32x2 res[5];
  const int cq = cb * 20 + g;
#pragma unroll
  for (int T = 0; T < 5; ++T) {
    const int q = cq + 4 * T;
    const char* rp = FINAL ? ws + OFF_X3 + (size_t)q * 16384 + s * 8
                           : ws + OFF_XB + (size_t)(s >> 5) * 40960 + (q >> 1) * 512 + (s & 31) * 16 + (q & 1) * 8;
    asm volatile("global_load_dwordx2 %0, %1, off" : "=v"(res[T]) : "v"(rp) : "memory");
  }
  f32x4 acc[5] = {};
  STAMP(1);
  const lds_ptr ta = smem + (16 * (g >> 1) + 4 * (g & 1) + (p >> 2)) * 160 + (p & 3) * 8;
#pragma unroll
  for (int m = 0; m < 5; ++m) {
    WAIT_BAR(5 + 9 * (4 - m));
    STAMP(2 + m);
#pragma unroll
    for (int i = 0; i < 4; ++i) PIN(of[4 * m + i]);
#pragma unroll
    for (int i = 0; i < 4; ++i) {
      const int ks = 4 * m + i;
      const bf16x8 b = __builtin_bit_cast(bf16x8, of[ks]);
#pragma unroll
      for (int T = 0; T < 5; ++T) {
        const bf16x8 a = cat8(lds_tr(ta + ks * 5120 + T * 32), lds_tr(ta + ks * 5120 + 1280 + T * 32));
        acc[T] = MFMA16(a, b, acc[T]);
      }
    }
  }
  WAITV(0);
#pragma unroll
  for (int T = 0; T < 5; ++T) PIN(res[T]);
  STAMP(7);
  float ssq = 0.f;
#pragma unroll
  for (int T = 0; T < 5; ++T) {
    const int q = cq + 4 * T;
    const float v0 = acc[T][0] + lo16(res[T][0]), v1 = acc[T][1] + hi16(res[T][0]);
    const float v2 = acc[T][2] + lo16(res[T][1]), v3 = acc[T][3] + hi16(res[T][1]);
    const u32x2 ov = {pack2(v0, v1), pack2(v2, v3)};
    if (FINAL) {
      *(u32x2*)(out + (size_t)s * 1280 + q * 8) = ov;
    } else {
      *(u32x2*)(ws + OFF_XA + SZ_X + (size_t)q * 16384 + s * 8) = ov;
      ssq += v0 * v0 + ((q == 0) ? 0.f : v1 * v1) + v2 * v2 + v3 * v3;
    }
  }
  if (!FINAL) {
    ssq += __shfl_xor(ssq, 16);
    ssq += __shfl_xor(ssq, 32);
    if (g == 0) atomicAdd((double*)(ws + OFF_SS2) + s, (double)ssq);
  }
  STAMP(8); WAITV(0); STAMP(9); STAMP_END(FINAL ? 6 : 3);
}

extern "C" void kernel_launch(void* const* d_in, const int* in_sizes, int n_in, void* d_out, int out_size, void* d_ws, size_t ws_size,
                              hipStream_t stream) {
  const char* x = (const char*)d_in[0];
  const char* wr = (const char*)d_in[1];
  const char* wi = (const char*)d_in[2];
  const char* wqkv = (const char*)d_in[4];
  const char* wo = (const char*)d_in[5];
  char* ws = (char*)d_ws;
  (void)hipFuncSetAttribute((const void*)k_pre, hipFuncAttributeMaxDynamicSharedMemorySize, 81920);
  (void)hipFuncSetAttribute((const void*)k_attn<0>, hipFuncAttributeMaxDynamicSharedMemorySize, ATT_LDS);
  (void)hipFuncSetAttribute((const void*)k_attn<1>, hipFuncAttributeMaxDynamicSharedMemorySize, ATT_LDS);
  (void)hipFuncSetAttribute((const void*)k_proj<0>, hipFuncAttributeMaxDynamicSharedMemorySize, 102400);
  (void)hipFuncSetAttribute((const void*)k_proj<1>, hipFuncAttributeMaxDynamicSharedMemorySize, 102400);
  k_pre<<<520, 256, 81920, stream>>>(x, wr, wi, wqkv, wo, ws);
  k_col<<<256, 256, 0, stream>>>(ws, 0);
  k_attn<0><<<256, 512, ATT_LDS, stream>>>(ws);
  k_proj<0><<<256, 256, 102400, stream>>>(ws, ws + OFF_WO, (char*)d_out);
  k_col<<<160, 256, 0, stream>>>(ws, 1);
  k_attn<1><<<256, 512, ATT_LDS, stream>>>(ws);
  k_proj<1><<<256, 256, 102400, stream>>>(ws, wo, (char*)d_out);
}
```

```cpp
#include <hip/hip_runtime.h>
#include <stdint.h>

typedef __attribute__((ext_vector_type(2))) __bf16 bf16x2;
typedef __attribute__((ext_vector_type(8))) __bf16 bf16x8;
typedef __attribute__((ext_vector_type(16))) __bf16 bf16x16;
typedef __attribute__((ext_vector_type(4))) short s16x4;
typedef __attribute__((ext_vector_type(2))) float f32x2;
typedef __attribute__((ext_vector_type(4))) float f32x4;
typedef __attribute__((ext_vector_type(8))) float f32x8;
typedef __attribute__((ext_vector_type(16))) float f32x16;
typedef __attribute__((ext_vector_type(2))) unsigned u32x2;
typedef __attribute__((ext_vector_type(4))) unsigned u32x4;
#define LAS __attribute__((address_space(3)))
#define GAS __attribute__((address_space(1)))
typedef LAS char* lds_ptr;
typedef GAS unsigned gu32;

#define SBAR() __builtin_amdgcn_sched_barrier(0)
#define PIN(x) asm volatile("" : "+v"(x))
#define WAITV(N) asm volatile("s_waitcnt vmcnt(%0)" ::"i"(N) : "memory")
#define WAIT_BAR(N) asm volatile("s_waitcnt vmcnt(%0) lgkmcnt(0)\n\ts_barrier" ::"i"(N) : "memory")
#define LBAR() asm volatile("s_waitcnt lgkmcnt(0)\n\ts_barrier" ::: "memory")
#define MFMA32(a, b, c) __builtin_amdgcn_mfma_f32_32x32x16_bf16(a, b, c, 0, 0, 0)
#define MFMA32K8(a, b, c) __builtin_amdgcn_mfma_f32_32x32x8bf16_1k(a, b, c, 0, 0, 0)
#define MFMA16(a, b, c) __builtin_amdgcn_mfma_f32_16x16x32_bf16(a, b, c, 0, 0, 0)
#define SMFI(a, b, c, idx) __builtin_amdgcn_smfmac_f32_32x32x32_bf16(a, b, c, idx, 0, 0)
#define BAR() asm volatile("s_barrier" ::: "memory")
#define RLX_AGENT __ATOMIC_RELAXED, __HIP_MEMORY_SCOPE_AGENT

#if defined(__HIP_DEVICE_COMPILE__) && __HIP_DEVICE_COMPILE__
#pragma clang attribute push(__attribute__((target("no-packed-fp32-ops"))), apply_to = function)
#endif
constexpr unsigned SZ_X = 2048u * 640 * 2;
constexpr unsigned OFF_FLAGS = 0;
constexpr unsigned OFF_RS = 65536;
constexpr unsigned OFF_SS0 = OFF_RS + 16384;
constexpr unsigned OFF_SS2 = OFF_SS0 + 16384;
constexpr unsigned OFF_XA = OFF_SS2 + 16384;
constexpr unsigned OFF_XB = OFF_XA + 2 * SZ_X;
constexpr unsigned OFF_OB = OFF_XB + 2 * SZ_X;
constexpr unsigned OFF_X3 = OFF_OB + 2 * SZ_X;
constexpr unsigned OFF_WF = OFF_X3 + SZ_X;
constexpr unsigned OFF_WQ = OFF_WF + SZ_X;
constexpr unsigned OFF_WO = OFF_WQ + 32 * 102400;
constexpr unsigned OFF_KV = OFF_WO + 2 * 819200;
constexpr int TILE_B = 2688;
constexpr unsigned SZ_KV = 32u * 64 * TILE_B;

#define PFG 128
#define BIDX ((int)__builtin_amdgcn_workgroup_id_x())
#define TIDX ((int)__builtin_amdgcn_workitem_id_x())
__device__ __forceinline__ int imin(int a, int b) { return a < b ? a : b; }
__device__ __forceinline__ bool wany(bool c) { return __builtin_amdgcn_ballot_w64(c) != 0; }
__device__ __forceinline__ float shfl(float v, int lane) {
  return __builtin_bit_cast(float, __builtin_amdgcn_ds_bpermute(lane << 2, __builtin_bit_cast(int, v)));
}
template <int CTRL>
__device__ __forceinline__ float dppadd(float v) {
  return v + __builtin_bit_cast(float, __builtin_amdgcn_update_dpp(0, __builtin_bit_cast(int, v), CTRL, 0xf, 0xf, true));
}
__device__ __forceinline__ float sum16(float v) {
  const auto r = __builtin_amdgcn_permlane16_swap(__builtin_bit_cast(unsigned, v), __builtin_bit_cast(unsigned, v), false, false);
  const unsigned a = r[0], b = r[1];
  return __builtin_bit_cast(float, a) + __builtin_bit_cast(float, b);
}
__device__ __forceinline__ float sum32(float v) {
  const auto r = __builtin_amdgcn_permlane32_swap(__builtin_bit_cast(unsigned, v), __builtin_bit_cast(unsigned, v), false, false);
  const unsigned a = r[0], b = r[1];
  return __builtin_bit_cast(float, a) + __builtin_bit_cast(float, b);
}
__device__ __forceinline__ float max32(float v) {
  const auto r = __builtin_amdgcn_permlane32_swap(__builtin_bit_cast(unsigned, v), __builtin_bit_cast(unsigned, v), false, false);
  const unsigned a = r[0], b = r[1];
  return __builtin_fmaxf(__builtin_bit_cast(float, a), __builtin_bit_cast(float, b));
}
__device__ __forceinline__ unsigned pack2(float a, float b) {
  f32x2 f = {a, b};
  return __builtin_bit_cast(unsigned, __builtin_convertvector(f, bf16x2));
}
__device__ __forceinline__ unsigned packn(float a, float b) {
  unsigned r;
  asm("s_nop 0\n\tv_cvt_pk_bf16_f32 %0, -%1, %2" : "=v"(r) : "v"(a), "v"(b));
  return r;
}
__device__ __forceinline__ unsigned xr_(unsigned a, unsigned m) {
  unsigned r;
  asm("v_xor_b32 %0, %2, %1" : "=v"(r) : "v"(a), "v"(m));
  return r;
}
__device__ __forceinline__ float u2f(unsigned u) { return __builtin_bit_cast(float, u); }
__device__ __forceinline__ float lo16(unsigned u) { return __builtin_bit_cast(float, u << 16); }
__device__ __forceinline__ float hi16(unsigned u) { return __builtin_bit_cast(float, u & 0xffff0000u); }
__device__ __forceinline__ void glds16s(const void* sbase, unsigned voff, unsigned lds_base) {
  unsigned sv;
  asm volatile("s_mov_b32 %0, m0\n\ts_mov_b32 m0, %3\n\ts_nop 0\n\tglobal_load_lds_dwordx4 %1, %2\n\ts_mov_b32 m0, %0"
               : "=&s"(sv) : "v"(voff), "s"(sbase), "s"(lds_base) : "memory");
}
__device__ __forceinline__ unsigned uni(unsigned v) { return __builtin_amdgcn_readfirstlane(v); }
__device__ __forceinline__ int wo_off(int j, int q) {
  const int cb = q / 20;
  return cb * 102400 + (j >> 5) * 5120 + (q - cb * 20) * 256 + (((j & 31) * 8) ^ ((q & 1) * 32 + (q & 2) * 64));
}
__device__ __forceinline__ int swap23(int j) { return (j & ~12) | ((j & 4) << 1) | ((j & 8) >> 1); }
__device__ __forceinline__ float sq2(unsigned v, float acc) {
  const float a = lo16(v), b = hi16(v);
  return __builtin_fmaf(a, a, __builtin_fmaf(b, b, acc));
}
#define GLOADS16(dst, voff, sbase, OFF) asm volatile("global_load_dwordx4 %0, %1, %2 offset:%3" : "=v"(dst) : "v"(voff), "s"(sbase), "i"(OFF) : "memory")
#define GLOADS8(dst, voff, sbase, OFF) asm volatile("global_load_dwordx2 %0, %1, %2 offset:%3" : "=v"(dst) : "v"(voff), "s"(sbase), "i"(OFF) : "memory")
#define GLOAD16(dst, ptr, OFF) asm volatile("global_load_dwordx4 %0, %1, off offset:%2" : "=v"(dst) : "v"(ptr), "i"(OFF) : "memory")
__device__ __forceinline__ bf16x8 lds_rd16(lds_ptr p) { return *(const LAS bf16x8*)p; }
__device__ __forceinline__ s16x4 lds_tr(lds_ptr p) {
  return __builtin_amdgcn_ds_read_tr16_b64_v4i16((LAS s16x4*)p);
}
__device__ __forceinline__ bf16x8 cat8(s16x4 a, s16x4 b) {
  typedef __attribute__((ext_vector_type(8))) short s16x8;
  s16x8 r = {a[0], a[1], a[2], a[3], b[0], b[1], b[2], b[3]};
  return __builtin_bit_cast(bf16x8, r);
}

template <bool TR>
__device__ __forceinline__ void k0_gemm(const char* src, int row0, int cg, int kind, char* ws, lds_ptr smem) {
  const int tid = TIDX, l = tid & 63, w = uni(tid >> 6);
  const unsigned lds0 = (unsigned)(uintptr_t)smem;
  const bool split = !TR && kind == 0;
  const int pw = split ? 2 : 4;
  const int kh = (w & pw) ? 1 : 0;
  const int tile = cg * pw + (w & (pw - 1));
  constexpr int KST = TR ? 64 * 3840 : 128;
  const char* sb = src + uni(TR ? row0 * 2 : row0 * 1280);
  auto voff = [&](int rg) -> unsigned {
    return TR ? (8 * rg + (l >> 3)) * 3840 + (((l & 7) ^ (((l >> 4) & 1) << 2)) << 4)
              : (8 * rg + (l >> 3)) * 1280 + (((l & 7) ^ ((4 * rg + (l >> 4)) & 7)) << 4);
  };
  auto stage = [&](int i, int rg, unsigned vo) {
    glds16s(sb + i * KST, vo, lds0 + (i * 8 + rg) * 1024);
    glds16s(sb + (i + 5) * KST, vo, lds0 + (i * 8 + 40 + rg) * 1024);
  };
  if (!split) {
    stage(0, w, voff(w)); stage(1, w, voff(w));
    if (tile >= 10) {
      stage(2, w, voff(w)); stage(3, w, voff(w)); stage(4, w, voff(w));
      WAIT_BAR(8); WAIT_BAR(6); WAIT_BAR(4); WAIT_BAR(2); WAIT_BAR(0); BAR();
      return;
    }
  } else if (w < 4) {
    const unsigned v0 = voff(w), v1 = v0 + 32 * 1280;
#pragma unroll
    for (int i = 0; i < 3; ++i) { stage(i, w, v0); stage(i, w + 4, v1); }
    WAIT_BAR(8);
    stage(3, w, v0); stage(3, w + 4, v1);
    WAIT_BAR(8);
    stage(4, w, v0); stage(4, w + 4, v1);
    WAIT_BAR(8);
    u32x4 v[5];
    const int row = row0 + (cg & 3) * 16 + (tid >> 4);
#pragma unroll
    for (int j = 0; j < 5; ++j) GLOADS16(v[j], row * 1280 + (tid & 15) * 16, src, j * 256);
    WAIT_BAR(9);
    WAIT_BAR(5);
    WAITV(0);
    float s = 0.f, q = 0.f;
#pragma unroll
    for (int j = 0; j < 5; ++j) {
      PIN(v[j]);
#pragma unroll
      for (int e = 0; e < 4; ++e) s += lo16(v[j][e]) + hi16(v[j][e]);
    }
    float mean = dppadd<0x140>(dppadd<0x141>(dppadd<0x4E>(dppadd<0xB1>(s)))) * (1.f / 640.f);
    PIN(mean);
    BAR();
#pragma unroll
    for (int j = 0; j < 5; ++j)
#pragma unroll
      for (int e = 0; e < 4; ++e) {
        const float a = lo16(v[j][e]) - mean, c = hi16(v[j][e]) - mean;
        q = __builtin_fmaf(a, a, __builtin_fmaf(c, c, q));
      }
    q = dppadd<0x140>(dppadd<0x141>(dppadd<0x4E>(dppadd<0xB1>(q))));
    if ((tid & 15) == 0 && cg < 4) *(double*)(ws + OFF_SS0 + row * 8) = (double)q;
    return;
  } else {
    __builtin_amdgcn_s_setprio(1);
  }
  const int h = l >> 5, r32 = l & 31;
  const int cp = 32 * tile + r32;
  float nu = (float)(cp >> 1), amp = (kh && (cp & 2)) ? -0.05590169943749474f : 0.05590169943749474f, ph = (cp & 1) ? 0.25f : 0.f;
  if (cp == 0) amp = 0.03952847075210474f;
  if (cp == 1) { amp = 0.07905694150420949f; nu = 160.f; ph = 0.125f; }
  float fa[16], fb[16];
  {
    const float st = nu * (1.f / 640.f), p0 = __builtin_fmaf(st, (float)(16 * h), ph);
    const float c1 = 2.f * __builtin_amdgcn_cosf(st);
#pragma unroll
    for (int j = 0; j < 16; ++j) {
      if ((j & 7) < 2) {
        fa[j] = amp * __builtin_amdgcn_cosf(__builtin_fmaf(st, (float)j, p0));
        fb[j] = amp * __builtin_amdgcn_cosf(__builtin_fmaf(st, (float)(j + 32), p0));
      } else {
        fa[j] = __builtin_fmaf(c1, fa[j - 1], -fa[j - 2]);
        fb[j] = __builtin_fmaf(c1, fb[j - 1], -fb[j - 2]);
      }
    }
  }
  const float co = 2.f * __builtin_amdgcn_cosf(nu * (1.f / 20.f));
  lds_ptr ra[4];
  if (!TR) {
    const int f = (r32 >> 1) & 7;
#pragma unroll
    for (int q = 0; q < 4; ++q) ra[q] = smem + kh * (40960 + 4096) + (r32 >> 3) * 1024 + (r32 & 7) * 128 + (((2 * q + h) ^ f) << 4);
  } else {
    const int gm = l >> 4, q = (l >> 2) & 3, p = l & 3;
#pragma unroll
    for (int R = 0; R < 2; ++R)
      ra[R] = smem + kh * 40960 + (8 * (gm >> 1) + q) * 128 + (((4 * (R ^ kh) + 2 * (gm & 1) + (p >> 1)) ^ (((q >> 1) & 1) << 2)) << 4) + (p & 1) * 8;
    ra[2] = ra[3] = ra[0];
  }
  const int c0 = 32 * tile + 4 * h;
  const int row = row0 + 32 * kh + r32;
  unsigned base, mo;
  if (TR) {
    const int which = row / 640, rem = row - which * 640, head = rem / 20, d = rem - head * 20;
    int e = d;
    if (which == 2) { e = (head & 1) ? ((d < 4) ? d + 16 : d - 4) : d; if (e < 16) e = swap23(e); }
    base = OFF_WQ + head * 102400 + ((e < 16) ? which * 1024 + e * 16 : 3072 + (which * 4 + e - 16) * 16);
    mo = 51200;
  } else if (kind == 0) {
    base = OFF_XA + row * 8;
    mo = 80 * 16384;
  } else {
    base = OFF_WO;
    mo = 4 * 102400;
  }
  unsigned oa[4];
#pragma unroll
  for (int p = 0; p < 4; ++p) {
    const int c = c0 + 8 * p;
    if (TR) oa[p] = base + (c >> 5) * 5120 + ((c >> 3) & 3) * 256 + ((c >> 2) & 1) * 8;
    else oa[p] = base + ((kind == 0) ? (c >> 2) * 16384 : wo_off(row, c >> 2));
    PIN(oa[p]);
  }
  char* wm = ws + mo;
  lds_ptr rb[4];
#pragma unroll
  for (int q = 0; q < 4; ++q) rb[q] = ra[q] + (kh ? -4096 : 4096);
  f32x16 aE[2] = {}, aO[2] = {};
  unsigned ie = 0x88888888u, io = 0xDDDDDDDDu;
  PIN(ie); PIN(io);
  bf16x8 bx[2][2][2];
  u32x4 pe[2], po[2];
  auto rdb = [&](int s, int R, int x) {
    if (!TR) return lds_rd16((R ? rb[2 * (s & 1) + x] : ra[2 * (s & 1) + x]) + (s >> 1) * 8192);
    const lds_ptr q = ra[R] + s * 4096 + x * 2048;
    return cat8(lds_tr(q), lds_tr(q + 512));
  };
#pragma unroll
  for (int i = 0; i < 4; ++i) { pe[0][i] = pack2(fa[4 * i], fa[4 * i + 2]); po[0][i] = pack2(fa[4 * i + 1], fa[4 * i + 3]); }
  PIN(pe[0]); PIN(po[0]);
  if (!split) { stage(2, w, voff(w)); stage(3, w, voff(w)); stage(4, w, voff(w)); }
  WAIT_BAR(8);
#pragma unroll
  for (int i = 0; i < 4; ++i) bx[0][i >> 1][i & 1] = rdb(0, i >> 1, i & 1);
#pragma unroll
  for (int s = 0; s < 10; ++s) {
    if ((s & 1) && s < 7) { WAITV(7 - s); BAR(); }
    if (s == 7) WAIT_BAR(0);
    float* cur = (s & 1) ? fb : fa;
    float* nxt = (s & 1) ? fa : fb;
#pragma unroll
    for (int i = 0; i < 4; ++i) {
      const int R = 1 - (i >> 1);
      const bf16x16 b = __builtin_shufflevector(bx[s & 1][R][0], bx[s & 1][R][1], 0, 1, 2, 3, 4, 5, 6, 7, 8, 9, 10, 11, 12, 13, 14, 15);
      if (i & 1) aO[R] = SMFI(__builtin_bit_cast(bf16x8, po[s & 1]), b, aO[R], io);
      else aE[R] = SMFI(__builtin_bit_cast(bf16x8, pe[s & 1]), b, aE[R], ie);
      SBAR();
      if (s < 9) {
        bx[(s + 1) & 1][R][i & 1] = rdb(s + 1, R, i & 1);
        unsigned e = pack2(nxt[4 * i], nxt[4 * i + 2]), o = pack2(nxt[4 * i + 1], nxt[4 * i + 3]);
        PIN(e); PIN(o);
        pe[(s + 1) & 1][i] = e; po[(s + 1) & 1][i] = o;
        if (s < 8) {
#pragma unroll
          for (int j = 4 * i; j < 4 * i + 4; ++j) { cur[j] = __builtin_fmaf(co, nxt[j], -cur[j]); PIN(cur[j]); }
        }
      }
      SBAR();
    }
  }
  f32x16 E = aE[0], O = aO[0];
  {
    const f32x16 sE = aE[1], sO = aO[1];
    const lds_ptr ex = smem + (w + (w >> 2)) * 8192 + l * 16;
#pragma unroll
    for (int i = 0; i < 4; ++i) {
      *(LAS f32x4*)(ex + i * 1024) = (f32x4){sE[4 * i], sE[4 * i + 1], sE[4 * i + 2], sE[4 * i + 3]};
      *(LAS f32x4*)(ex + 4096 + i * 1024) = (f32x4){sO[4 * i], sO[4 * i + 1], sO[4 * i + 2], sO[4 * i + 3]};
    }
    LBAR();
    const lds_ptr ey = smem + ((w ^ pw) + ((w ^ pw) >> 2)) * 8192 + l * 16;
#pragma unroll
    for (int i = 0; i < 4; ++i) {
      const f32x4 u = *(const LAS f32x4*)(ey + i * 1024), v = *(const LAS f32x4*)(ey + 4096 + i * 1024);
#pragma unroll
      for (int j = 0; j < 4; ++j) { E[4 * i + j] += u[j]; O[4 * i + j] += v[j]; }
    }
  }
  f32x16 P, M;
#pragma unroll
  for (int r = 0; r < 16; ++r) { P[r] = E[r] + O[r]; M[r] = (r & 1) ? O[r] - E[r] : E[r] - O[r]; }
  if (c0 == 0) { P[1] = TR ? 0.f : P[0]; P[0] = M[0]; M[0] = E[1]; M[1] = O[1]; }
#pragma unroll
  for (int p = 0; p < 4; ++p) {
    *(u32x2*)(ws + oa[p]) = (u32x2){pack2(P[4 * p], P[4 * p + 1]), pack2(P[4 * p + 2], P[4 * p + 3])};
    *(u32x2*)(wm + oa[p]) = (u32x2){pack2(M[4 * p], M[4 * p + 1]), pack2(M[4 * p + 2], M[4 * p + 3])};
  }
}

__global__ __launch_bounds__(512, 2) void k_pre(const char* __restrict__ x, const char* __restrict__ wr, const char* __restrict__ wi,
                                                const char* __restrict__ wqkv, const char* __restrict__ wo, char* __restrict__ ws) {
  extern __shared__ __attribute__((aligned(16))) char smem_[];
  lds_ptr smem = (lds_ptr)smem_;
  const int bid = BIDX, tid = TIDX;
  if (bid < 190) {
    const bool isx = bid < 160;
    const int i = isx ? bid >> 3 : bid - 160, nc = isx ? 5 : 3;
    k0_gemm<false>(isx ? x : wo, ((isx ? (bid & 7) * 4 : 0) + i / nc) * 64, i % nc, isx ? 0 : 2, ws, smem);
  } else if (bid >= 192) {
    const int b = bid - 192, k2b = b & 7, k1b = b >> 3;
    const unsigned lds0 = (unsigned)(uintptr_t)smem;
    const int w = uni(tid >> 6), l = tid & 63;
    {
      const char* sg = (w < 4 ? wr : wi) + uni((k1b * 8 + 64 * (4 * k2b + (w & 3))) * 642);
#pragma unroll
      for (int i = 0; i < 5; ++i) glds16s(sg + i * 1024, l * 16, lds0 + w * 5136 + i * 1024);
      if (l < 1) glds16s(sg + 5120, l * 16, lds0 + w * 5136 + 5120);
    }
    u32x4 t[5];
#pragma unroll
    for (int i = 0; i < 5; ++i) GLOADS16(t[i], imin(b * 38400 + (i * 512 + tid) * 16, 2457600 - 16), wqkv, 0);
    WAIT_BAR(5);
    if (tid < 96) *(u32x4*)(ws + (tid < 80 ? OFF_FLAGS + (b * 80 + tid) * 16 : OFF_SS2 + (b * 16 + tid - 80) * 16)) = (u32x4){0u, 0u, 0u, 0u};
    const LAS unsigned short* L = (const LAS unsigned short*)smem + (tid & 7) * 321;
    const __amdgpu_buffer_rsrc_t wfr = __builtin_amdgcn_make_buffer_rsrc(ws + OFF_WF, 0, SZ_X, 0x00020000);
#pragma unroll
    for (int rd = 0; rd < 5; ++rd) {
      const int c = rd * 64 + ((l >> 3) * 8 + w);
      const int k2 = c == 0 ? 320 : (c <= 160 ? c : 480 - c);
      u32x4 o;
#pragma unroll
      for (int i = 0; i < 4; ++i) o[i] = (unsigned)L[i * 2568 + k2] | ((unsigned)L[10272 + i * 2568 + k2] << 16);
      __builtin_amdgcn_raw_buffer_store_b128(o, wfr, c * 8192 + (k1b >> 1) * 2048 + k2b * 256 + ((k1b & 1) * 8 + (tid & 7)) * 16, 0, 16);
    }
    WAITV(0);
#pragma unroll
    for (int i = 0; i < 5; ++i) asm volatile("" ::"v"(t[i]));
  }
}

__global__ __launch_bounds__(512, 2) void k_col(char* __restrict__ ws, const char* __restrict__ wsrc, int layer) {
  extern __shared__ __attribute__((aligned(16))) char smem_[];
  lds_ptr smem = (lds_ptr)smem_;
  const int bid = BIDX, tid = TIDX;
  if (bid >= 160 && layer == 0) {
    const int i = bid - 160, rb = (i & 7) * 4 + (i >> 3) / 3;
    if (rb < 30) k0_gemm<true>(wsrc, rb * 64, (i >> 3) % 3, 1, ws, smem);
    return;
  }
  if (bid >= 160) {
    for (int it = (bid - 160) * 512 + tid; it < 51200; it += 96 * 512) {
      const int j = it / 80, c16 = it - j * 80;
      const u32x4 wv = *(const u32x4*)(wsrc + it * 16);
      const int row = it % 20, ch = (it / 20) % 80, head = it / 1600;
      const int n = head * 20 + row;
      u32x4 o;
#pragma unroll
      for (int i = 0; i < 4; ++i) {
        const int sl = ch * 4 + i, c = sl <= 160 ? sl : 480 - sl;
        const float rev = (float)((c * n) % 640) * (1.f / 640.f);
        float a = 0.05590169943749474f * __builtin_amdgcn_cosf(rev), b = -0.05590169943749474f * __builtin_amdgcn_sinf(rev);
        if (c == 0) { b = 0.03952847075210474f; a = (n & 1) ? -b : b; }
        o[i] = pack2(a, b);
      }
      *(u32x4*)(ws + OFF_WQ + head * 102400 + (ch >> 2) * 5120 + (ch & 3) * 256 + (row < 16 ? 4096 + row * 16 : 3072 + (row - 4) * 16)) = o;
      *(u32x2*)(ws + OFF_WO + 819200 + wo_off(j, 2 * c16)) = (u32x2){wv[0], wv[1]};
      *(u32x2*)(ws + OFF_WO + 819200 + wo_off(j, 2 * c16 + 1)) = (u32x2){wv[2], wv[3]};
    }
    return;
  }
  const int u = ((bid & 7) * 20 + (bid >> 3));
  const int l = tid & 63, w = uni(tid >> 6), p = l & 15, g = l >> 4, q4 = w & 3, hi = w >> 2;
  u32x4 wf[2];
  const unsigned lds0 = (unsigned)(uintptr_t)smem;
  {
    const unsigned vo = (l >> 3) * 256 + (l & 7) * 16;
    const char* xa = ws + uni(OFF_XA + layer * SZ_X + u * 16384 + q4 * 4096 + hi * 128);
    const char* ss = ws + uni((layer ? OFF_SS2 : OFF_SS0) + q4 * 4096 + hi * 128);
    glds16s(xa, vo, lds0 + 32768 + w * 4096);
    glds16s(xa + 2048, vo, lds0 + 32768 + w * 4096 + 1024);
    glds16s(ss, vo, lds0 + 32768 + w * 4096 + 2048);
    glds16s(ss + 2048, vo, lds0 + 32768 + w * 4096 + 3072);
    const char* wp = ws + uni(OFF_WF + (2 * u + hi) * 8192 + q4 * 2048);
    GLOADS16(wf[0], l * 16, wp, 0);
    GLOADS16(wf[1], l * 16, wp, 1024);
  }
  u32x4 b1r[4], b1i[4], a2r[2][2], a2i[2][2];
  float twr[8], twi[8];
  float m64 = (float)(16 * q4 + p) * (1.f / 64.f), x0 = (float)(4 * g);
  PIN(m64); PIN(x0);
  float a64 = m64 * x0;
  {
    float p32 = (float)p * (1.f / 32.f);
    unsigned odd = (p & 1) ? 0x80008000u : 0u, s1r = (p & 2) ? 0x80008000u : 0u, s1i = ((p + 1) & 2) ? 0x80008000u : 0u, eve = odd ^ 0x80008000u;
    PIN(p32); PIN(odd); PIN(s1r); PIN(s1i); PIN(eve);
    const float a32 = p32 * x0;
#pragma unroll
    for (int i = 0; i < 4; ++i) {
      const float rv = __builtin_fmaf(m64, (float)i, a64), cs = __builtin_amdgcn_cosf(rv), sn = __builtin_amdgcn_sinf(rv);
      const unsigned re = pack2(cs, sn), im = packn(sn, cs);
      b1r[0][i] = re; b1i[0][i] = im;
      b1r[1][i] = ((p & 1) ? im : re) ^ s1r; b1i[1][i] = ((p & 1) ? re : im) ^ s1i;
      b1r[2][i] = re ^ odd; b1i[2][i] = im ^ odd;
      b1r[3][i] = b1r[1][i] ^ odd; b1i[3][i] = b1i[1][i] ^ odd;
      const float rw = __builtin_fmaf(p32, (float)i, a32), c2 = __builtin_amdgcn_cosf(rw), s2 = __builtin_amdgcn_sinf(rw);
      const unsigned r2 = pack2(c2, s2), i2 = packn(s2, c2);
      a2r[0][0][i] = r2; a2i[0][0][i] = i2;
      a2r[0][1][i] = r2 ^ odd; a2i[0][1][i] = i2 ^ odd;
      a2r[1][0][i] = xr_(r2, (i & 1) ? 0x80008000u : 0u); a2i[1][0][i] = xr_(i2, (i & 1) ? 0x80008000u : 0u);
      a2r[1][1][i] = xr_(r2, (i & 1) ? eve : odd); a2i[1][1][i] = xr_(i2, (i & 1) ? eve : odd);
    }
#pragma unroll
    for (int k = 0; k < 4; ++k) { PIN(b1r[k]); PIN(b1i[k]); PIN(a2r[k >> 1][k & 1]); PIN(a2i[k >> 1][k & 1]); }
  }
  lds_ptr rd1[4], rd4[4], wu[4];
  unsigned ob = OFF_XB + layer * SZ_X + (2 * (16 * q4 + 4 * g) + hi) * 20480 + (u >> 1) * 256 + p * 16 + (u & 1) * 8;
#pragma unroll
  for (int k = 0; k < 4; ++k) {
    const lds_ptr b = smem + p * 256 + (((4 * k + g) ^ p) << 4);
    rd1[k] = b + hi * 8192;
    rd4[k] = b + hi * 4096;
    wu[k] = smem + hi * 8192 + (4 * g + k) * 256 + (((4 * q4 + (p >> 2)) ^ (4 * g + k)) << 4) + (p & 3) * 4;
    PIN(rd1[k]); PIN(rd4[k]); PIN(wu[k]);
  }
  PIN(ob);
  const float mask0 = (u == 0) ? 0.f : 1.f;
  WAITV(2);
  u32x2 xr[4];
  {
    u32x4 z0, z1;
    u32x2 sv[4];
    const lds_ptr pr = smem + 32768 + w * 4096 + g * 512 + p * 8;
#pragma unroll
    for (int r = 0; r < 4; ++r) { sv[r] = *(const LAS u32x2*)(pr + 2048 + r * 128); xr[r] = *(const LAS u32x2*)(pr + r * 128); }
#pragma unroll
    for (int r = 0; r < 4; ++r) {
      const float rs = __builtin_amdgcn_rsqf(__builtin_fmaf((float)__builtin_bit_cast(double, sv[r]), 1.f / 640.f, 1e-5f));
      z0[r] = pack2(lo16(xr[r][0]) * rs, hi16(xr[r][0]) * (rs * mask0));
      z1[r] = pack2(lo16(xr[r][1]) * rs, hi16(xr[r][1]) * rs);
    }
    const lds_ptr zp = smem + (16 * hi + p) * 256 + (((4 * q4 + g) ^ p) << 4);
    *(LAS u32x4*)(zp) = z0;
    *(LAS u32x4*)(zp + 8192) = z1;
  }
  LBAR();
  {
    bf16x8 a[2][4];
#pragma unroll
    for (int k = 0; k < 4; ++k)
#pragma unroll
      for (int t = 0; t < 2; ++t) a[t][k] = lds_rd16(rd1[k] + t * 4096);
    SBAR();
    float fr[8], fi[8];
    WAITV(0);
    PIN(wf[0]); PIN(wf[1]);
#pragma unroll
    for (int i = 0; i < 8; ++i) {
      const float rev = __builtin_fmaf(m64, (float)(16 * (i >> 2) + (i & 3)), a64) * (1.f / 32.f);
      twr[i] = __builtin_amdgcn_cosf(rev);
      twi[i] = __builtin_amdgcn_sinf(-rev);
      fr[i] = lo16(wf[i >> 2][i & 3]); fi[i] = hi16(wf[i >> 2][i & 3]);
    }
    f32x4 tr[2] = {}, ti[2] = {};
#pragma unroll
    for (int k = 0; k < 4; ++k)
#pragma unroll
      for (int t = 0; t < 2; ++t) {
        tr[t] = MFMA16(a[t][k], __builtin_bit_cast(bf16x8, b1r[k]), tr[t]);
        ti[t] = MFMA16(a[t][k], __builtin_bit_cast(bf16x8, b1i[k]), ti[t]);
      }
    u32x4 bb[2];
#pragma unroll
    for (int i = 0; i < 8; ++i) {
      const float x = tr[i >> 2][i & 3], y = ti[i >> 2][i & 3];
      bb[i >> 2][i & 3] = pack2(x * twr[i] - y * twi[i], x * twi[i] + y * twr[i]);
    }
    f32x4 xr2[2] = {}, xi2[2] = {};
#pragma unroll
    for (int t = 0; t < 2; ++t)
#pragma unroll
      for (int t2 = 0; t2 < 2; ++t2) {
        xr2[t2] = MFMA16(__builtin_bit_cast(bf16x8, a2r[t2][t]), __builtin_bit_cast(bf16x8, bb[t]), xr2[t2]);
        xi2[t2] = MFMA16(__builtin_bit_cast(bf16x8, a2i[t2][t]), __builtin_bit_cast(bf16x8, bb[t]), xi2[t2]);
      }
#pragma unroll
    for (int i = 0; i < 8; ++i) {
      const float x = xr2[i >> 2][i & 3], y = xi2[i >> 2][i & 3];
      bb[i >> 2][i & 3] = pack2(x * fr[i] - y * fi[i], __builtin_fmaf(-x, fi[i], -y * fr[i]));
    }
    f32x4 ur[2] = {}, ui[2] = {};
#pragma unroll
    for (int t = 0; t < 2; ++t)
#pragma unroll
      for (int t2 = 0; t2 < 2; ++t2) {
        ur[t2] = MFMA16(__builtin_bit_cast(bf16x8, a2r[t2][t]), __builtin_bit_cast(bf16x8, bb[t]), ur[t2]);
        ui[t2] = MFMA16(__builtin_bit_cast(bf16x8, a2i[t2][t]), __builtin_bit_cast(bf16x8, bb[t]), ui[t2]);
      }
#pragma unroll
    for (int i = 0; i < 8; ++i) {
      const float x = ur[i >> 2][i & 3], y = ui[i >> 2][i & 3];
      *(LAS unsigned*)(wu[i & 3] + 16384 + (i >> 2) * 4096) =
          pack2(x * twr[i] - y * twi[i], x * twi[i] + y * twr[i]);
    }
  }
  LBAR();
  f32x4 yr[2] = {}, yi[2] = {};
  {
    bf16x8 b[2][4];
#pragma unroll
    for (int k = 0; k < 4; ++k)
#pragma unroll
      for (int c = 0; c < 2; ++c) b[c][k] = lds_rd16(rd4[k] + 16384 + c * 8192);
    SBAR();
#pragma unroll
    for (int k = 0; k < 4; ++k)
#pragma unroll
      for (int c = 0; c < 2; ++c) {
        yr[c] = MFMA16(__builtin_bit_cast(bf16x8, b1r[k]), b[c][k], yr[c]);
        yi[c] = MFMA16(__builtin_bit_cast(bf16x8, b1i[k]), b[c][k], yi[c]);
      }
  }
  const float sc = 1.f / 2048.f, sc0 = -sc * mask0;
#pragma unroll
  for (int r = 0; r < 4; ++r) {
    const u32x2 o = {pack2(__builtin_fmaf(yr[0][r], sc, lo16(xr[r][0])), __builtin_fmaf(yi[0][r], sc0, hi16(xr[r][0]))),
                     pack2(__builtin_fmaf(yr[1][r], sc, lo16(xr[r][1])), __builtin_fmaf(yi[1][r], -sc, hi16(xr[r][1])))};
    *(u32x2*)(ws + r * 40960 + ob) = o;
  }
}

constexpr int POS_B = 4 * TILE_B;
constexpr int ATT_LDS = 163840;
__host__ __device__ constexpr int slot_off(int pos) { return ((pos + 10) % 15) * POS_B; }
#define EXP2(x) __builtin_amdgcn_exp2f(x)
#define MX3(a, b, c) __builtin_fmaxf(__builtin_fmaxf((a), (b)), (c))
#define SMF(a, b, c) __builtin_amdgcn_smfmac_f32_32x32x32_bf16(a, __builtin_bit_cast(bf16x16, b), c, sidx, 0, 0)
#define A_QB(S, K, Q) "v_mfma_f32_32x32x16_bf16 " S ", " K ", " Q ", 0\n\t"
#define A_QA(S, K, Q) "v_mfma_f32_32x32x8_bf16 " S ", " K ", " Q ", " S "\n\t"
#define A_PV(O, V, P) "v_smfmac_f32_32x32x32_bf16 " O ", " V ", " P ", v112\n\t"
#define A_E4(d0, d1, d2, d3, s0, s1, s2, s3) \
  "v_exp_f32 v" #d0 ", v" #s0 "\n\tv_exp_f32 v" #d1 ", v" #s1 "\n\tv_exp_f32 v" #d2 ", v" #s2 "\n\tv_exp_f32 v" #d3 ", v" #s3 "\n\t"
#define A_PAIR(KC1, KC2, KN1, KN2, KOFF, V1RD, V0RD)                                          \
  A_QB("v[48:63]", KC1, "v[84:87]") A_E4(64, 65, 66, 67, 32, 33, 34, 35)                      \
  A_QA("v[48:63]", KC2, "v[90:91]")                                                           \
  "ds_read_b128 " KC1 ", v113 offset:" KOFF "\n\tds_read_b64 " KC2 ", v114 offset:" KOFF "\n\t" \
  A_E4(68, 69, 70, 71, 36, 37, 38, 39)                                                        \
  A_PV("v[16:31]", "v[108:111]", "v[72:79]") "ds_read_b128 v[108:111], " V1RD "\n\t"          \
  A_E4(72, 73, 74, 75, 40, 41, 42, 43)                                                        \
  "s_waitcnt lgkmcnt(3)\n\t" A_PV("v[0:15]", "v[104:107]", "v[64:71]") A_E4(76, 77, 78, 79, 44, 45, 46, 47) \
  A_QB("v[32:47]", KN1, "v[80:83]") A_E4(64, 65, 66, 67, 48, 49, 50, 51)                      \
  A_QA("v[32:47]", KN2, "v[88:89]") A_E4(68, 69, 70, 71, 52, 53, 54, 55)                      \
  "s_waitcnt lgkmcnt(0)\n\t" A_PV("v[0:15]", "v[108:111]", "v[72:79]") A_E4(72, 73, 74, 75, 56, 57, 58, 59) \
  A_PV("v[16:31]", "v[104:107]", "v[64:71]") "ds_read_b128 v[104:107], " V0RD "\n\t"          \
  A_E4(76, 77, 78, 79, 60, 61, 62, 63)

template <int LAYER>
__global__ __launch_bounds__(512, 2) void k_attn(char* __restrict__ ws) {
  extern __shared__ __attribute__((aligned(16))) char smem_[];
  lds_ptr smem = (lds_ptr)smem_;
  const unsigned lds0 = (unsigned)(uintptr_t)smem;
  const int bid = BIDX, tid = TIDX, l = tid & 63, w = uni(tid >> 6);
  const int h = l >> 5, r32 = l & 31;
  const int head = ((bid & 7) >> 1) * 8 + ((bid >> 3) & 7), qb = (bid & 1) * 4 + (bid >> 6);
  const bool early = w < 4;
  constexpr int NT = LAYER ? 5 : 4;
  const int p = l & 15, g = l >> 4;
  const unsigned lo = l * 16;
  const char* xp = ws + uni(OFF_XB + LAYER * SZ_X + (qb * 8 + w) * 40960);
#define PSW 13
#define LPRIO 14
#define XRAMP 4
  constexpr int XS = 10 + 2 * XRAMP;
  u32x4 xf[XS];
  LAS unsigned* cnts = (LAS unsigned*)(smem + 15 * POS_B + 2048);
  gu32* rsg = (gu32*)(ws + OFF_RS) + LAYER * 2048 + qb * 256;
#define XLOAD(j) GLOADS16(xf[(j) % XS], lo, xp + ((j) & 1) * 20480 + ((j) >> 3) * 4096, (((j) >> 1) & 3) * 1024)
  unsigned cad = lds0 + 15 * POS_B + 2048, one = 1u;
  PIN(cad); PIN(one);
#define signal(I) asm volatile("s_mov_b64 exec, 1\n\tds_add_u32 %0, %1 offset:%2\n\ts_mov_b64 exec, -1" ::"v"(cad), "v"(one), "i"(4 * (I)) : "memory")
#define await(I, N) do { unsigned t_, u_;                                                                                              \
    asm volatile("1:\n\tds_read_b32 %0, %2 offset:%3\n\ts_waitcnt lgkmcnt(0)\n\tv_readfirstlane_b32 %1, %0\n\ts_cmp_ge_u32 %1, %4\n\t" \
                 "s_cbranch_scc1 2f\n\ts_sleep 1\n\ts_branch 1b\n2:"                                                                   \
                 : "=&v"(t_), "=&s"(u_) : "v"(cad), "i"(4 * (I)), "i"(N) : "scc", "memory"); } while (0)
  const char* gw = ws + uni(OFF_WQ + head * 102400 + w * 5120);
  auto stage = [&](int d) {
    unsigned sm;
    asm volatile("s_mov_b32 %0, m0\n\ts_mov_b32 m0, %3\n\ts_nop 0\n\t"
                 "global_load_lds_dwordx4 %1, %2\n\tglobal_load_lds_dwordx4 %1, %2 offset:1024\n\t"
                 "global_load_lds_dwordx4 %1, %2 offset:2048\n\tglobal_load_lds_dwordx4 %1, %2 offset:3072\n\ts_mov_b32 m0, %0"
                 : "=&s"(sm) : "v"(lo), "s"(gw + d * 5120), "s"(lds0 + (w + d) * 5120) : "memory");
    if (LAYER) glds16s(gw + d * 5120 + 4096, lo, lds0 + (w + d) * 5120 + 4096);
  };
  stage(0);
  if (tid < 32) cnts[tid] = 0u;
  LBAR();
  if (w == 7) {
    u32x4 sv[10];
    const char* sp = ws + uni(OFF_XB + LAYER * SZ_X + (qb * 16 + (head >> 1)) * 20480 + (head & 1) * 128);
    const unsigned so_ = (l & 7) * 16 + (l >> 3) * 256;
#pragma unroll
    for (int i = 0; i < 10; ++i) GLOADS16(sv[i], so_, sp + (i >> 1) * 4096, (i & 1) * 2048);
    WAITV(10);
    signal(1);
    float q0 = 0.f, q1 = 0.f;
#pragma unroll
    for (int i = 0; i < 10; ++i) {
      WAITV(9 - i);
      PIN(sv[i]);
      if (i == 0 && l < 8) sv[0][0] &= 0xffffu;
      q0 = sq2(sv[i][1], sq2(sv[i][0], q0));
      q1 = sq2(sv[i][3], sq2(sv[i][2], q1));
    }
    const float q = sum32(sum16(dppadd<0x128>(q0 + q1)));
    const float r = __builtin_amdgcn_rsqf(__builtin_fmaf(q, 1.f / 640.f, 1e-5f));
    if (l < 8) __hip_atomic_store(rsg + head * 8 + l, __builtin_bit_cast(unsigned, r) | 0x80000000u, RLX_AGENT);
  } else if (!early) {
    stage(4);
    WAITV(NT);
    signal(1);
    stage(7);
    stage(10);
    stage(13);
    WAITV(0);
    signal(2);
  }
#pragma unroll
  for (int j = 0; j < 8; ++j) XLOAD(j);
  const lds_ptr mine = smem + slot_off(0) + w * TILE_B;
  const lds_ptr q1s = smem + 102400 + (w & 3) * 1024;
  const lds_ptr q2s = smem + 15 * POS_B + (w & 3) * 512;
  lds_ptr wq1 = q1s + (g >> 1) * 512 + p * 16 + (g & 1) * 8, wk1 = wq1 + (mine - q1s);
  lds_ptr wv = mine + 1280 + (g >> 1) * 352 + p * 16 + (g & 1) * 8;
  lds_ptr we = h ? mine + 1280 + (p >> 3) * 352 + (p & 7) * 2 + 256 : (g ? mine + 1024 : q2s) + p * 8;
  lds_ptr rq2 = h ? mine + 1280 + 21 * 16 : q2s + r32 * 8, rv2 = mine + imin(2048 + l * 16, TILE_B - 16);
  const int k1o = l * 16, k2o = h ? 1280 + 20 * 16 : 1024 + r32 * 8, vo = 1280 + h * 352 + (r32 < 21 ? r32 : 21) * 16;
  lds_ptr rk1 = mine + k1o, rk2 = mine + k2o;
  gu32* rsa = rsg + w * 32 + p;
  gu32* rsb = rsg + w * 32 + 4 * g;
  float qsc = g ? -1.f : -0.22360679774997896f * 1.4426950408889634f;
  PIN(wq1); PIN(wk1); PIN(wv); PIN(we); PIN(rq2); PIN(rv2); PIN(rk1); PIN(rk2); PIN(rsa); PIN(rsb); PIN(qsc);
  if (l < 8) {
    const unsigned c1 = (l & 1) ? 0u : 0x3f803f80u;
    *(LAS u32x4*)(mine + 1280 + (l >> 1) * 352 + (20 + (l & 1)) * 16) = (u32x4){c1, c1, c1, c1};
  }
  if (early) { WAITV(8); signal(0); }
  await(0, 4);
  f32x4 acc[NT][2] = {};
  unsigned rsu[2];
  u32x4 rsv[2];
  u32x2 e3[2];
  {
    const lds_ptr wa = smem + l * 16;
    bf16x8 a[3][NT];
#pragma unroll
    for (int n = 0; n < NT; ++n) { a[0][n] = lds_rd16(wa + n * 1024); a[1][n] = lds_rd16(wa + 5120 + n * 1024); }
#pragma unroll
    for (int k2 = 0; k2 < 20; ++k2) {
      const int is0 = k2 < XRAMP ? 8 + 4 * k2 : 8 + 2 * XRAMP + 2 * k2, issued = is0 < 40 ? is0 : 40;
      if (k2 == 2) await(1, 4);
      if (k2 == 6) await(2, 3);
      if (k2 == LPRIO && !early) __builtin_amdgcn_s_setprio(1);
      WAITV(issued - 2 * k2 - 2 + (k2 > 14 ? 4 : 0));
      PIN(xf[(2 * k2) % XS]); PIN(xf[(2 * k2 + 1) % XS]);
      SBAR();
#pragma unroll
      for (int n = 0; n < NT; ++n)
#pragma unroll
        for (int c = 0; c < 2; ++c) {
          const bf16x8 b = __builtin_bit_cast(bf16x8, xf[(2 * k2 + c) % XS]);
          acc[n][c] = (n == 2) ? MFMA16(b, a[k2 % 3][n], acc[n][c]) : MFMA16(a[k2 % 3][n], b, acc[n][c]);
          SBAR();
          if (c == 0 && k2 + 2 < 20) a[(k2 + 2) % 3][n] = lds_rd16(wa + (k2 + 2) * 5120 + n * 1024);
          if (c == 1 && n < 4) {
            if (k2 < XRAMP) { if (is0 + n < 40) XLOAD(is0 + n); }
            else if (!(n & 1) && is0 < 40) XLOAD(is0 + (n >> 1));
            if (k2 == 14 && n < 2) {
              asm volatile("global_load_dword %0, %1, off offset:%2 sc1" : "=v"(rsu[n]) : "v"(rsa), "i"(64 * n) : "memory");
              asm volatile("global_load_dwordx4 %0, %1, off offset:%2 sc1" : "=v"(rsv[n]) : "v"(rsb), "i"(64 * n) : "memory");
            }
          }
          SBAR();
        }
    }
  }
  asm volatile("s_waitcnt vmcnt(0)" : "+v"(rsv[0]), "+v"(rsv[1])::"memory");
  unsigned rb[8] = {rsv[0][0], rsv[0][1], rsv[0][2], rsv[0][3], rsv[1][0], rsv[1][1], rsv[1][2], rsv[1][3]};
  {
    unsigned tmp, spins = 1u << 20;
    asm volatile("s_waitcnt vmcnt(0)\n1:\n\t"
                 "v_bitop3_b32 %[t], %[a0], %[a1], %[b0] bitop3:0x80\n\t"
                 "v_bitop3_b32 %[t], %[t], %[b1], %[b2] bitop3:0x80\n\t"
                 "v_bitop3_b32 %[t], %[t], %[b3], %[c0] bitop3:0x80\n\t"
                 "v_bitop3_b32 %[t], %[t], %[c1], %[c2] bitop3:0x80\n\t"
                 "v_and_b32 %[t], %[t], %[c3]\n\t"
                 "v_cmp_le_i32 vcc, 0, %[t]\n\t"
                 "s_cbranch_vccz 2f\n\t"
                 "s_sleep 1\n\t"
                 "global_load_dword %[a0], %[pa], off sc1\n\tglobal_load_dword %[a1], %[pa], off offset:64 sc1\n\t"
                 "global_load_dword %[b0], %[pb], off sc1\n\tglobal_load_dword %[b1], %[pb], off offset:4 sc1\n\t"
                 "global_load_dword %[b2], %[pb], off offset:8 sc1\n\tglobal_load_dword %[b3], %[pb], off offset:12 sc1\n\t"
                 "global_load_dword %[c0], %[pb], off offset:64 sc1\n\tglobal_load_dword %[c1], %[pb], off offset:68 sc1\n\t"
                 "global_load_dword %[c2], %[pb], off offset:72 sc1\n\tglobal_load_dword %[c3], %[pb], off offset:76 sc1\n\t"
                 "s_sub_u32 %[n], %[n], 1\n\ts_cmp_lg_u32 %[n], 0\n\ts_waitcnt vmcnt(0)\n\ts_cbranch_scc1 1b\n2:"
                 : [t] "=&v"(tmp), [n] "+s"(spins), [a0] "+v"(rsu[0]), [a1] "+v"(rsu[1]), [b0] "+v"(rb[0]), [b1] "+v"(rb[1]), [b2] "+v"(rb[2]),
                   [b3] "+v"(rb[3]), [c0] "+v"(rb[4]), [c1] "+v"(rb[5]), [c2] "+v"(rb[6]), [c3] "+v"(rb[7])
                 : [pa] "v"(rsa), [pb] "v"(rsb) : "vcc", "scc", "memory");
  }
  if (!early) await(4, 4);
#pragma unroll
  for (int c = 0; c < 2; ++c) {
    const float rs = -u2f(rsu[c]);
    const float qs = rs * (0.22360679774997896f * 1.4426950408889634f), s3 = u2f(rsu[c]) * qsc;
    *(LAS u32x2*)(wq1 + c * 256) = (u32x2){pack2(acc[0][c][0] * qs, acc[0][c][1] * qs), pack2(acc[0][c][2] * qs, acc[0][c][3] * qs)};
    *(LAS u32x2*)(wk1 + c * 256) = (u32x2){pack2(acc[1][c][0] * rs, acc[1][c][1] * rs), pack2(acc[1][c][2] * rs, acc[1][c][3] * rs)};
    e3[c] = (u32x2){pack2(acc[3][c][0] * s3, acc[3][c][1] * s3), pack2(acc[3][c][2] * s3, acc[3][c][3] * s3)};
  }
  if (!h) { *(LAS u32x2*)(we) = e3[0]; *(LAS u32x2*)(we + 128) = e3[1]; }
  asm volatile("" ::: "memory");
  u32x4 qa1 = *(const LAS u32x4*)(q1s + l * 16);
  u32x2 qa2 = *(const LAS u32x2*)(rq2);
  char* kvh = ws + uni(OFF_KV + LAYER * SZ_KV + head * 64 * TILE_B);
  {
    const u32x4 k1u = *(const LAS u32x4*)(rk1);
    const s16x4 k2u = *(const LAS s16x4*)(rk2);
    SBAR();
#pragma unroll
    for (int c = 0; c < 2; ++c)
      *(LAS u32x2*)(wv + c * 704) = (u32x2){pack2(acc[2][c][0] * -u2f(rb[4 * c]), acc[2][c][1] * -u2f(rb[4 * c + 1])),
                                            pack2(acc[2][c][2] * -u2f(rb[4 * c + 2]), acc[2][c][3] * -u2f(rb[4 * c + 3]))};
    if (g == 2) {
#pragma unroll
      for (int c = 0; c < 2; ++c) {
        *(LAS unsigned short*)(we + c * 704) = (unsigned short)e3[c][0];
        *(LAS unsigned short*)(we + c * 704 + 16) = (unsigned short)(e3[c][0] >> 16);
        *(LAS unsigned short*)(we + c * 704 + 32) = (unsigned short)e3[c][1];
        *(LAS unsigned short*)(we + c * 704 + 48) = (unsigned short)(e3[c][1] >> 16);
      }
    }
    asm volatile("" ::: "memory");
    const u32x4 v1 = *(const LAS u32x4*)(rk1 + 1024);
    const u32x4 v2 = *(const LAS u32x4*)(rv2);
    f32x16 s = {};
    s = MFMA32(__builtin_bit_cast(bf16x8, k1u), __builtin_bit_cast(bf16x8, qa1), s);
    s = MFMA32K8(k2u, __builtin_bit_cast(s16x4, qa2), s);
    const __amdgpu_buffer_rsrc_t rsrc = __builtin_amdgcn_make_buffer_rsrc(kvh + (unsigned)((qb * 8 + w) * TILE_B), 0, TILE_B, 0x00020000);
    __builtin_amdgcn_raw_buffer_store_b128(k1u, rsrc, l * 16, 0, 16);
    __builtin_amdgcn_raw_buffer_store_b128(v1, rsrc, 1024 + l * 16, 0, 16);
    __builtin_amdgcn_raw_buffer_store_b128(v2, rsrc, 2048 + l * 16, 0, 16);
    float m0 = MX3(s[0], s[1], s[2]), m1 = MX3(s[3], s[4], s[5]);
    m0 = MX3(m0, s[6], s[7]); m1 = MX3(m1, s[8], s[9]);
    m0 = MX3(m0, s[10], s[11]); m1 = MX3(m1, s[12], s[13]);
    m0 = MX3(m0, s[14], s[15]);
    m0 = __builtin_fmaxf(m0, m1);
    const float m = max32(m0);
    const float mh = hi16(pack2(0.f, -m));
    if (h) {
      qa2[0] = pack2(mh, -m - mh);
      *(LAS u32x2*)(q2s + l * 8) = qa2;
    }
  }
  if (early) signal(3); else signal(5);
  if (LAYER) {
#pragma unroll
    for (int c = 0; c < 2; ++c) {
      char* x3 = ws + (OFF_X3 + (5 * head + g) * 16384 + (qb * 256 + w * 32 + 16 * c + p) * 8);
      *(u32x2*)(x3) = (u32x2){pack2(acc[NT - 1][c][0], acc[NT - 1][c][1]), pack2(acc[NT - 1][c][2], acc[NT - 1][c][3])};
      if (g == 3) *(u32x2*)(x3 + 16384) = (u32x2){pack2(acc[3][c][0], acc[3][c][1]), pack2(acc[3][c][2], acc[3][c][3])};
    }
  }
  gu32* fl = (gu32*)(ws + OFF_FLAGS + (size_t)(LAYER * 256 + head * 8 + w) * 128);
  auto publish = [&]() {
    WAITV(0);
    if (l == 0) __hip_atomic_store(fl + qb, 1u, RLX_AGENT);
  };
  unsigned nxt = 1, rdy = 0, dst = lds0 + (early ? slot_off(2) : slot_off(9)) + (w & 3) * TILE_B;
  gu32* fp = fl + ((qb + l) & 7);
  auto fetch = [&](int n) {
#pragma unroll 1
    for (unsigned spins = 0; n > 0; ++spins) {
      if (!((rdy >> nxt) & 1)) rdy = (unsigned)__builtin_amdgcn_ballot_w64(__hip_atomic_load(fp, RLX_AGENT) == 1u);
      if (spins > (1u << 20)) rdy = ~0u;
#pragma unroll 1
      for (; ((rdy >> nxt) & 1) && n > 0; --n) {
        unsigned sv;
        asm volatile("s_mov_b32 %0, m0\n\ts_mov_b32 m0, %3\n\ts_nop 0\n\t"
                     "global_load_lds_dwordx4 %1, %2 sc1\n\tglobal_load_lds_dwordx4 %1, %2 offset:1024 sc1\n\t"
                     "s_mov_b32 exec_hi, 0xff\n\tglobal_load_lds_dwordx4 %1, %2 offset:2048 sc1\n\ts_mov_b32 exec_hi, -1\n\ts_mov_b32 m0, %0"
                     : "=&s"(sv) : "v"(k1o), "s"(kvh + uni((((qb + nxt) & 7) * 8 + w) * TILE_B)), "s"(dst) : "memory");
        dst += POS_B;
        ++nxt;
      }
    }
  };
  unsigned sidx = 0xDDDDDDDDu;
  u32x4 v1 = {};
  f32x16 oA, oB, sB, pp;
  asm volatile("s_nop 1\n\tv_mfma_f32_32x32x8_bf16 v[0:15], v[108:109], v[108:109], 0\n\tv_mfma_f32_32x32x8_bf16 v[16:31], v[108:109], v[108:109], 0\n\t"
               "v_mfma_f32_32x32x8_bf16 v[64:79], v[108:109], v[108:109], 0"
               : "={v[0:15]}"(oA), "={v[16:31]}"(oB), "={v[48:63]}"(sB), "={v[64:79]}"(pp), "+{v[108:111]}"(v1));
  const int kof = (w & 1) * 2 * TILE_B + (int)lds0;
  const int bk1 = kof + k1o, bk2 = kof + k2o, bv = kof + vo;
  if (early) await(3, 4); else await(5, 4);
  u32x4 qb1 = *(const LAS u32x4*)(smem + 102400 + ((w & 3) ^ 1) * 1024 + l * 16);
  u32x2 qb2 = *(const LAS u32x2*)(smem + 15 * POS_B + ((w & 3) ^ 1) * 512 + l * 8);
  u32x4 kA1, kB1, v0;
  u32x2 kA2, kB2;
  f32x16 sA;
  unsigned vnext = bv + slot_off(0);
  unsigned so = slot_off(0);
  unsigned exa = lds0 + w * 4096 + l * 16, fla = cad + 64 + 4 * w;
  unsigned ob1 = OFF_OB + LAYER * SZ_X + (qb * 16 + 2 * w + (r32 >> 4)) * 20480 + p * 16 + ((5 * head) >> 1) * 256;
  unsigned ob2 = ob1 + ((head & 1) ? 8 : 512);
  ob1 += ((head & 1) + h) * 256;
  PIN(exa); PIN(fla); PIN(ob1); PIN(ob2);
#pragma unroll 1
  for (int seg = 0; seg < 6; ++seg) {
    int cnt = ((early ? 0x080071 : (0x003221 | ((PSW - 8) << 16) | ((16 - PSW) << 20))) >> (4 * seg)) & 15;
    if (cnt == 0) continue;
    if (seg == 1) {
      signal(8);
      if (early) {
        fetch(3);
        await(5, 4);
        dst = lds0 + slot_off(5) + (w & 3) * TILE_B;
        fetch(4);
        WAITV(12); signal(6);
        WAITV(0); signal(7);
        await(7, 4);
      } else {
        publish();
        await(6, 4);
      }
    } else if (seg == 2) {
      await(8, 8);
      fetch(7);
      await(7, 4);
    } else if (seg == 3) {
      WAITV(0);
      signal(9);
    } else if (seg == 4) {
      signal(10);
      await(9, 4);
    } else if (seg == 5) {
      __builtin_amdgcn_s_setprio(0);
    }
    if (seg == 0 || (seg == 1 && early)) {
      asm volatile("v_add_u32 v113, %[so], %[bk1]\n\tv_add_u32 v114, %[so], %[bk2]\n\t"
                   "ds_read_b128 v[92:95], v113\n\tds_read_b64 v[100:101], v114\n\tds_read_b128 v[104:107], v116\n\t"
                   "ds_read_b128 v[96:99], v113 offset:2688\n\tds_read_b64 v[102:103], v114 offset:2688\n\t"
                   "s_waitcnt lgkmcnt(3)\n\t" A_QB("v[32:47]", "v[92:95]", "v[80:83]") A_QA("v[32:47]", "v[100:101]", "v[88:89]")
                   "s_waitcnt lgkmcnt(0)\n\ts_nop 3"
                   : "={v[32:47]}"(sA), "+{v[80:83]}"(qa1), "+{v[84:87]}"(qb1), "+{v[88:89]}"(qa2), "+{v[90:91]}"(qb2), "={v[92:95]}"(kA1),
                     "={v[96:99]}"(kB1), "={v[100:101]}"(kA2), "={v[102:103]}"(kB2), "={v[104:107]}"(v0), "+{v116}"(vnext)
                   : [so] "s"(so), [bk1] "v"(bk1), [bk2] "v"(bk2) : "v113", "v114", "memory");
      if (early && seg == 0) { signal(4); publish(); }
    }
    asm volatile(
        "1:\n\t"
        "s_add_u32 %[so], %[so], %[pos]\n\t"
        "s_cmp_eq_u32 %[so], %[lim]\n\t"
        "s_cselect_b32 %[so], 0, %[so]\n\t"
        "v_mov_b32 v115, v116\n\t"
        "v_add_u32 v113, %[so], %[bk1]\n\t"
        "v_add_u32 v114, %[so], %[bk2]\n\t"
        "v_add_u32 v116, %[so], %[bv]\n\t"
        A_PAIR("v[92:95]", "v[100:101]", "v[96:99]", "v[102:103]", "0", "v115 offset:704", "v115 offset:2688")
        A_PAIR("v[96:99]", "v[102:103]", "v[92:95]", "v[100:101]", "2688", "v115 offset:3392", "v116")
        "s_sub_u32 %[cnt], %[cnt], 1\n\t"
        "s_cmp_lg_u32 %[cnt], 0\n\t"
        "s_cbranch_scc1 1b\n\t"
        "s_waitcnt lgkmcnt(0)"
        : "+{v[0:15]}"(oA), "+{v[16:31]}"(oB), "+{v[32:47]}"(sA), "+{v[48:63]}"(sB), "+{v[64:79]}"(pp), "+{v[80:83]}"(qa1),
          "+{v[84:87]}"(qb1), "+{v[88:89]}"(qa2), "+{v[90:91]}"(qb2), "+{v[92:95]}"(kA1), "+{v[96:99]}"(kB1), "+{v[100:101]}"(kA2),
          "+{v[102:103]}"(kB2), "+{v[104:107]}"(v0), "+{v[108:111]}"(v1), "+{v112}"(sidx), "+{v116}"(vnext), [so] "+s"(so), [cnt] "+s"(cnt)
        : [bk1] "v"(bk1), [bk2] "v"(bk2), [bv] "v"(bv), [pos] "i"(POS_B), [lim] "i"(15 * POS_B)
        : "scc", "v113", "v114", "v115", "memory");
  }
  f32x4 e0, e1, e2;
  {
    unsigned t_, u_;
    asm volatile(A_PV("v[16:31]", "v[108:111]", "v[72:79]")
                 "1:\n\tds_read_b32 %[t], %[cad] offset:40\n\ts_waitcnt lgkmcnt(0)\n\tv_readfirstlane_b32 %[u], %[t]\n\t"
                 "s_cmp_ge_u32 %[u], 8\n\ts_cbranch_scc0 1b\n\ts_nop 7\n\t"
                 "ds_write_b128 %[ex], v[16:19]\n\tds_write_b128 %[ex], v[20:23] offset:1024\n\tds_write_b128 %[ex], v[24:27] offset:2048\n\t"
                 "ds_write_b32 %[fw], %[one]\n"
                 "2:\n\tds_read_b32 %[t], %[fr]\n\tds_read_b128 %[e0], %[ey]\n\tds_read_b128 %[e1], %[ey] offset:1024\n\t"
                 "ds_read_b128 %[e2], %[ey] offset:2048\n\ts_waitcnt lgkmcnt(0)\n\tv_readfirstlane_b32 %[u], %[t]\n\t"
                 "s_cmp_lg_u32 %[u], 0\n\ts_cbranch_scc1 3f\n\ts_sleep 1\n\ts_branch 2b\n3:"
                 : "+{v[0:15]}"(oA), "+{v[16:31]}"(oB), "+{v[64:79]}"(pp), "+{v[108:111]}"(v1), "+{v112}"(sidx), [t] "=&v"(t_), [u] "=&s"(u_),
                   [e0] "=&v"(e0), [e1] "=&v"(e1), [e2] "=&v"(e2)
                 : [cad] "v"(cad), [one] "v"(one), [ex] "v"(exa), [ey] "v"(exa ^ 4096u), [fw] "v"(fla), [fr] "v"(fla ^ 4u)
                 : "scc", "memory");
  }
  auto finish = [&](const f32x16& o) {
    const auto sw = __builtin_amdgcn_permlane32_swap(__builtin_bit_cast(unsigned, (float)o[8]), __builtin_bit_cast(unsigned, (float)o[8]), false, false);
    const float ls = u2f(sw[1]), il = __builtin_amdgcn_rcpf(ls);
    *(u32x4*)(ws + ob1) = (u32x4){pack2(o[0] * il, o[1] * il), pack2(o[2] * il, o[3] * il), pack2(o[4] * il, o[5] * il), pack2(o[6] * il, o[7] * il)};
    if (!h) *(u32x2*)(ws + ob2) = (u32x2){pack2(o[8] * il, o[9] * il), pack2(o[10] * il, o[11] * il)};
    return ls;
  };
#pragma unroll
  for (int j = 0; j < 4; ++j) { oA[j] += e0[j]; oA[4 + j] += e1[j]; oA[8 + j] += e2[j]; }
  const float lsum = finish(oA);
  float chk = lsum;
#pragma unroll
  for (int r = 0; r < 12; ++r) chk += __builtin_fabsf(oA[r]);
  if (__builtin_expect(wany(!(chk < 1e30f) || !(lsum > 1e-20f)), 0)) {
    __builtin_amdgcn_fence(__ATOMIC_ACQUIRE, "agent");
    float m = -__builtin_inff();
    f32x16 o = {};
    const u32x2 q2z = {h ? 0u : qa2[0], qa2[1]};
#pragma unroll 1
    for (int t = 0; t < 64; ++t) {
      const char* g = kvh + t * TILE_B;
      const bf16x8 k1 = *(const bf16x8*)(g + k1o), w0 = *(const bf16x8*)(g + vo), w1 = *(const bf16x8*)(g + vo + 704);
      const s16x4 k2 = *(const s16x4*)(g + k2o);
      f32x16 s = {};
      s = MFMA32K8(k2, __builtin_bit_cast(s16x4, q2z), s);
      s = MFMA32(k1, __builtin_bit_cast(bf16x8, qa1), s);
      float mn = m;
#pragma unroll
      for (int r = 0; r < 16; ++r) mn = __builtin_fmaxf(mn, s[r]);
      mn = max32(mn);
      const float al = EXP2(m - mn);
      m = mn;
      f32x8 pl, ph;
#pragma unroll
      for (int r = 0; r < 16; ++r) o[r] *= al;
#pragma unroll
      for (int r = 0; r < 8; ++r) { pl[r] = EXP2(s[r] - m); ph[r] = EXP2(s[r + 8] - m); }
      o = SMF(w0, pl, o);
      o = SMF(w1, ph, o);
    }
    finish(o);
  }
}

template <int FINAL>
__global__ __launch_bounds__(512, 2) void k_proj(char* __restrict__ ws, char* __restrict__ out) {
  extern __shared__ __attribute__((aligned(16))) char smem_[];
  lds_ptr smem = (lds_ptr)smem_;
  const unsigned lds0 = (unsigned)(uintptr_t)smem;
  const int bid = BIDX, tid = TIDX, l = tid & 63, w = uni(tid >> 6);
  const int rb = (bid & 7) * 4 + ((bid >> 3) & 3), cb = bid >> 5;
  const int p = l & 15, g = l >> 4, st = w & 3, kh = w >> 2;
  const unsigned lo = l * 16;
  const char* wsrc = ws + uni(OFF_WO + FINAL * 819200 + cb * 102400 + kh * 51200 + st * 2560);
  const char* osrc = ws + uni(OFF_OB + FINAL * SZ_X + (rb * 4 + st) * 20480 + kh * 10240);
  const unsigned wdst = lds0 + kh * 51200 + st * 2560;
  u32x4 of[10];
  auto stage = [&](int s) {
    unsigned sv;
    asm volatile("s_mov_b32 %0, m0\n\ts_mov_b32 m0, %3\n\ts_nop 0\n\t"
                 "global_load_lds_dwordx4 %1, %2\n\tglobal_load_lds_dwordx4 %1, %2 offset:1024\n\t"
                 "s_mov_b32 exec_hi, 0\n\tglobal_load_lds_dwordx4 %1, %2 offset:2048\n\ts_mov_b32 exec_hi, -1\n\ts_mov_b32 m0, %0"
                 : "=&s"(sv) : "v"(lo), "s"(wsrc + s * 10240), "s"(wdst + s * 10240) : "memory");
    GLOADS16(of[2 * s], lo, osrc + s * 2048, 0);
    GLOADS16(of[2 * s + 1], lo, osrc + s * 2048, 1024);
  };
  stage(0); stage(1); stage(2);
  const int s = rb * 64 + st * 16 + p;
  const int cq = cb * 20 + kh * 12 + g;
  u32x2 res[3];
  f32x4 acc[5] = {};
  const lds_ptr ta = smem + kh * 51200 + wo_off(8 * g + (p >> 2), p & 3), tb = smem + kh * 51200 + wo_off(8 * g + 4 + (p >> 2), p & 3);
#pragma unroll
  for (int m = 0; m < 5; ++m) {
    if (m < 3) WAIT_BAR(10); else if (m == 3) WAIT_BAR(8); else WAIT_BAR(3);
    bf16x8 a[2][5];
#pragma unroll
    for (int i = 0; i < 2; ++i)
#pragma unroll
      for (int T = 0; T < 5; ++T) a[i][T] = cat8(lds_tr(ta + (2 * m + i) * 5120 + T * 1024), lds_tr(tb + (2 * m + i) * 5120 + T * 1024));
    SBAR();
    if (m < 2) stage(m + 3);
    if (m == 2) {
#pragma unroll
      for (int T = 0; T < 3; ++T) {
        const int q = imin(cq + 4 * T, cb * 20 + 19);
        const char* rp = ws + (FINAL ? OFF_X3 + q * 16384 + s * 8 : OFF_XB + (s >> 4) * 20480 + (q >> 1) * 256 + (s & 15) * 16 + (q & 1) * 8);
        asm volatile("global_load_dwordx2 %0, %1, off" : "=v"(res[T]) : "v"(rp) : "memory");
      }
    }
    PIN(of[2 * m]); PIN(of[2 * m + 1]);
#pragma unroll
    for (int i = 0; i < 2; ++i)
#pragma unroll
      for (int T = 0; T < 5; ++T) acc[T] = MFMA16(a[i][T], __builtin_bit_cast(bf16x8, of[2 * m + i]), acc[T]);
  }
  f32x4 fin[3];
  {
    const lds_ptr ex = smem + 102400 + w * 3072 + l * 16;
    *(LAS f32x4*)(ex) = kh ? acc[0] : acc[3];
    *(LAS f32x4*)(ex + 1024) = kh ? acc[1] : acc[4];
    *(LAS f32x4*)(ex + 2048) = acc[2];
    WAIT_BAR(0);
    const lds_ptr ey = smem + 102400 + (w ^ 4) * 3072 + l * 16;
    fin[0] = (kh ? acc[3] : acc[0]) + *(const LAS f32x4*)(ey);
    fin[1] = (kh ? acc[4] : acc[1]) + *(const LAS f32x4*)(ey + 1024);
    fin[2] = acc[2] + *(const LAS f32x4*)(ey + 2048);
  }
  float ssq = 0.f;
#pragma unroll
  for (int T = 0; T < 3; ++T) {
    PIN(res[T]);
    if (T == 2 && kh) break;
    const int q = cq + 4 * T;
    const float v0 = fin[T][0] + lo16(res[T][0]), v1 = fin[T][1] + hi16(res[T][0]);
    const float v2 = fin[T][2] + lo16(res[T][1]), v3 = fin[T][3] + hi16(res[T][1]);
    const u32x2 ov = {pack2(v0, v1), pack2(v2, v3)};
    if (FINAL) {
      *(u32x2*)(out + s * 1280 + q * 8) = ov;
    } else {
      *(u32x2*)(ws + (OFF_XA + SZ_X + q * 16384 + s * 8)) = ov;
      ssq += v0 * v0 + ((q == 0) ? 0.f : v1 * v1) + v2 * v2 + v3 * v3;
    }
  }
  if (!FINAL) {
    ssq = sum32(sum16(ssq));
    if (g == 0) __hip_atomic_fetch_add((GAS double*)(ws + OFF_SS2) + s, (double)ssq, RLX_AGENT);
  }
}

#if defined(__HIP_DEVICE_COMPILE__) && __HIP_DEVICE_COMPILE__
#pragma clang attribute pop
#endif

extern "C" void kernel_launch(void* const* d_in, const int* in_sizes, int n_in, void* d_out, int out_size, void* d_ws, size_t ws_size,
                              hipStream_t stream) {
  const char* x = (const char*)d_in[0];
  const char* wr = (const char*)d_in[1];
  const char* wi = (const char*)d_in[2];
  const char* wqkv = (const char*)d_in[4];
  const char* wo = (const char*)d_in[5];
  char* ws = (char*)d_ws;
  constexpr int PROJ_LDS = 102400 + 8 * 3072;
  (void)hipFuncSetAttribute((const void*)k_pre, hipFuncAttributeMaxDynamicSharedMemorySize, 83968);
  (void)hipFuncSetAttribute((const void*)k_col, hipFuncAttributeMaxDynamicSharedMemorySize, 83968);
  (void)hipFuncSetAttribute((const void*)k_attn<0>, hipFuncAttributeMaxDynamicSharedMemorySize, ATT_LDS);
  (void)hipFuncSetAttribute((const void*)k_attn<1>, hipFuncAttributeMaxDynamicSharedMemorySize, ATT_LDS);
  (void)hipFuncSetAttribute((const void*)k_proj<0>, hipFuncAttributeMaxDynamicSharedMemorySize, PROJ_LDS);
  (void)hipFuncSetAttribute((const void*)k_proj<1>, hipFuncAttributeMaxDynamicSharedMemorySize, PROJ_LDS);
  k_pre<<<256, 512, 83968, stream>>>(x, wr, wi, wqkv, wo, ws);
  k_col<<<256, 512, 83968, stream>>>(ws, wqkv, 0);
  k_attn<0><<<256, 512, ATT_LDS, stream>>>(ws);
  k_proj<0><<<256, 512, PROJ_LDS, stream>>>(ws, (char*)d_out);
  k_col<<<256, 512, 83968, stream>>>(ws, wo, 1);
  k_attn<1><<<256, 512, ATT_LDS, stream>>>(ws);
  k_proj<1><<<256, 512, PROJ_LDS, stream>>>(ws, (char*)d_out);
}
```

```cpp
#include <hip/hip_runtime.h>
#include <stdint.h>

typedef __attribute__((ext_vector_type(2))) __bf16 bf16x2;
typedef __attribute__((ext_vector_type(8))) __bf16 bf16x8;
typedef __attribute__((ext_vector_type(16))) __bf16 bf16x16;
typedef __attribute__((ext_vector_type(4))) short s16x4;
typedef __attribute__((ext_vector_type(2))) float f32x2;
typedef __attribute__((ext_vector_type(4))) float f32x4;
typedef __attribute__((ext_vector_type(8))) float f32x8;
typedef __attribute__((ext_vector_type(16))) float f32x16;
typedef __attribute__((ext_vector_type(2))) unsigned u32x2;
typedef __attribute__((ext_vector_type(4))) unsigned u32x4;
#define LAS __attribute__((address_space(3)))
#define GAS __attribute__((address_space(1)))
typedef LAS char* lds_ptr;
typedef GAS unsigned gu32;

#define SBAR() __builtin_amdgcn_sched_barrier(0)
#define PIN(x) asm volatile("" : "+v"(x))
#define WAITV(N) asm volatile("s_waitcnt vmcnt(%0)" ::"i"(N) : "memory")
#define WAIT_BAR(N) asm volatile("s_waitcnt vmcnt(%0) lgkmcnt(0)\n\ts_barrier" ::"i"(N) : "memory")
#define LBAR() asm volatile("s_waitcnt lgkmcnt(0)\n\ts_barrier" ::: "memory")
#define MFMA32(a, b, c) __builtin_amdgcn_mfma_f32_32x32x16_bf16(a, b, c, 0, 0, 0)
#define MFMA32K8(a, b, c) __builtin_amdgcn_mfma_f32_32x32x8bf16_1k(a, b, c, 0, 0, 0)
#define MFMA16(a, b, c) __builtin_amdgcn_mfma_f32_16x16x32_bf16(a, b, c, 0, 0, 0)
#define SMFI(a, b, c, idx) __builtin_amdgcn_smfmac_f32_32x32x32_bf16(a, b, c, idx, 0, 0)
#define BAR() asm volatile("s_barrier" ::: "memory")
#define RLX_AGENT __ATOMIC_RELAXED, __HIP_MEMORY_SCOPE_AGENT

#if defined(__HIP_DEVICE_COMPILE__) && __HIP_DEVICE_COMPILE__
#pragma clang attribute push(__attribute__((target("no-packed-fp32-ops"))), apply_to = function)
#endif
constexpr unsigned SZ_X = 2048u * 640 * 2;
constexpr unsigned OFF_FLAGS = 0;
constexpr unsigned OFF_GC = 262144;
constexpr unsigned OFF_OF = OFF_GC + 327680;
constexpr unsigned OFF_RS = OFF_OF + 131072;
constexpr unsigned OFF_SS2 = OFF_RS + 16384;
constexpr unsigned SZ_ZERO = OFF_SS2 + 16384;
constexpr unsigned OFF_BEA = SZ_ZERO;
constexpr unsigned OFF_SS0 = OFF_BEA + 8192;
constexpr unsigned OFF_XA = OFF_SS0 + 16384;
constexpr unsigned OFF_XB = OFF_XA + 2 * SZ_X;
constexpr unsigned OFF_OB = OFF_XB + 2 * SZ_X;
constexpr unsigned OFF_X3 = OFF_OB + 2 * SZ_X;
constexpr unsigned OFF_WF = OFF_X3 + SZ_X;
constexpr unsigned OFF_WQ = OFF_WF + SZ_X;
constexpr unsigned OFF_WO = OFF_WQ + 32 * 102400;
constexpr unsigned OFF_KV = OFF_WO + 2 * 819200;
constexpr int TILE_B = 2688;
constexpr unsigned SZ_KV = 32u * 64 * TILE_B;

#define SPIN_MAX (1u << 17)
#define BIDX ((int)__builtin_amdgcn_workgroup_id_x())
#define TIDX ((int)__builtin_amdgcn_workitem_id_x())
__device__ __forceinline__ int imin(int a, int b) { return a < b ? a : b; }
__device__ __forceinline__ bool wany(bool c) { return __builtin_amdgcn_ballot_w64(c) != 0; }
template <int CTRL>
__device__ __forceinline__ float dppadd(float v) {
  return v + __builtin_bit_cast(float, __builtin_amdgcn_update_dpp(0, __builtin_bit_cast(int, v), CTRL, 0xf, 0xf, true));
}
__device__ __forceinline__ float sum16(float v) {
  const auto r = __builtin_amdgcn_permlane16_swap(__builtin_bit_cast(unsigned, v), __builtin_bit_cast(unsigned, v), false, false);
  const unsigned a = r[0], b = r[1];
  return __builtin_bit_cast(float, a) + __builtin_bit_cast(float, b);
}
__device__ __forceinline__ float sum32(float v) {
  const auto r = __builtin_amdgcn_permlane32_swap(__builtin_bit_cast(unsigned, v), __builtin_bit_cast(unsigned, v), false, false);
  const unsigned a = r[0], b = r[1];
  return __builtin_bit_cast(float, a) + __builtin_bit_cast(float, b);
}
__device__ __forceinline__ float max32(float v) {
  const auto r = __builtin_amdgcn_permlane32_swap(__builtin_bit_cast(unsigned, v), __builtin_bit_cast(unsigned, v), false, false);
  const unsigned a = r[0], b = r[1];
  return __builtin_fmaxf(__builtin_bit_cast(float, a), __builtin_bit_cast(float, b));
}
__device__ __forceinline__ unsigned pack2(float a, float b) {
  f32x2 f = {a, b};
  return __builtin_bit_cast(unsigned, __builtin_convertvector(f, bf16x2));
}
__device__ __forceinline__ unsigned packn(float a, float b) {
  unsigned r;
  asm("s_nop 0\n\tv_cvt_pk_bf16_f32 %0, -%1, %2" : "=v"(r) : "v"(a), "v"(b));
  return r;
}
__device__ __forceinline__ unsigned xr_(unsigned a, unsigned m) {
  unsigned r;
  asm("v_xor_b32 %0, %2, %1" : "=v"(r) : "v"(a), "v"(m));
  return r;
}
__device__ __forceinline__ float u2f(unsigned u) { return __builtin_bit_cast(float, u); }
__device__ __forceinline__ float lo16(unsigned u) { return __builtin_bit_cast(float, u << 16); }
__device__ __forceinline__ float hi16(unsigned u) { return __builtin_bit_cast(float, u & 0xffff0000u); }
__device__ __forceinline__ void glds16s(const void* sbase, unsigned voff, unsigned lds_base) {
  unsigned sv;
  asm volatile("s_mov_b32 %0, m0\n\ts_mov_b32 m0, %3\n\ts_nop 0\n\tglobal_load_lds_dwordx4 %1, %2\n\ts_mov_b32 m0, %0"
               : "=&s"(sv) : "v"(voff), "s"(sbase), "s"(lds_base) : "memory");
}
__device__ __forceinline__ void glds16c(const void* sbase, unsigned voff, unsigned lds_base) {
  unsigned sv;
  asm volatile("s_mov_b32 %0, m0\n\ts_mov_b32 m0, %3\n\ts_nop 0\n\tglobal_load_lds_dwordx4 %1, %2 sc1\n\ts_mov_b32 m0, %0"
               : "=&s"(sv) : "v"(voff), "s"(sbase), "s"(lds_base) : "memory");
}
__device__ __forceinline__ unsigned uni(unsigned v) { return __builtin_amdgcn_readfirstlane(v); }
__device__ __forceinline__ int swap23(int j) { return (j & ~12) | ((j & 4) << 1) | ((j & 8) >> 1); }
__device__ __forceinline__ float sq2(unsigned v, float acc) {
  const float a = lo16(v), b = hi16(v);
  return __builtin_fmaf(a, a, __builtin_fmaf(b, b, acc));
}
#define GLOADC16(dst, voff, sbase, OFF) asm volatile("global_load_dwordx4 %0, %1, %2 offset:%3 sc1" : "=v"(dst) : "v"(voff), "s"(sbase), "i"(OFF) : "memory")
__device__ __forceinline__ bf16x8 lds_rd16(lds_ptr p) { return *(const LAS bf16x8*)p; }
__device__ __forceinline__ s16x4 lds_tr(lds_ptr p) {
  return __builtin_amdgcn_ds_read_tr16_b64_v4i16((LAS s16x4*)p);
}
__device__ __forceinline__ bf16x8 cat8(s16x4 a, s16x4 b) {
  typedef __attribute__((ext_vector_type(8))) short s16x8;
  s16x8 r = {a[0], a[1], a[2], a[3], b[0], b[1], b[2], b[3]};
  return __builtin_bit_cast(bf16x8, r);
}

__device__ __forceinline__ void gadd(char* ws, int id, int bid, int l) {
  __hip_atomic_fetch_add((gu32*)(ws + OFF_GC) + ((id * 64 + l) * 8 + (bid & 7)) * 32, 1u, RLX_AGENT);
}
__device__ __forceinline__ void gsignal(char* ws, int id, int bid, int tid) {
  WAIT_BAR(0);
  if (tid < 64) gadd(ws, id, bid, tid);
}
__device__ __forceinline__ void gpoll(char* ws, int id, unsigned n, int bid, int l) {
  gu32* c = (gu32*)(ws + OFF_GC) + ((id * 64 + (bid & 63)) * 8 + (l & 7)) * 32;
#pragma unroll 1
  for (unsigned spins = 0; spins < SPIN_MAX; ++spins) {
    if (!wany(__hip_atomic_load(c, RLX_AGENT) < n)) break;
    __builtin_amdgcn_s_sleep(1);
  }
}

__device__ __forceinline__ void beacon_wait(gu32* bea, unsigned b0) {
#pragma unroll 1
  for (unsigned spins = 0; spins < SPIN_MAX; ++spins) {
    if (uni(__hip_atomic_load(bea, RLX_AGENT)) != b0) break;
    __builtin_amdgcn_s_sleep(1);
  }
}

template <bool TR>
__device__ __forceinline__ void dft_stage(const char* src, int row0, unsigned lds, int lw, int l, int i) {
  constexpr int KST = TR ? 64 * 3840 : 128;
  const char* sb = src + uni(TR ? row0 * 2 : row0 * 1280) + i * KST;
  const unsigned v0 = TR ? (8 * lw + (l >> 3)) * 3840 + (((l & 7) ^ (((l >> 4) & 1) << 2)) << 4)
                         : (8 * lw + (l >> 3)) * 1280 + (((l & 7) ^ ((4 * lw + (l >> 4)) & 7)) << 4);
  const unsigned v1 = v0 + 32 * (TR ? 3840 : 1280);
  lds += (i * 8 + lw) * 1024;
  glds16s(sb, v0, lds); glds16s(sb + 5 * KST, v0, lds + 40960);
  glds16s(sb, v1, lds + 4096); glds16s(sb + 5 * KST, v1, lds + 45056);
}
template <int KIND>
__device__ __forceinline__ void dft_compute(int row0, int cg, char* ws, lds_ptr smem, lds_ptr exb, int cw, int l) {
  constexpr bool TR = KIND == 1;
  const int kh = cw >> 1, tile = cg * 2 + (cw & 1);
  const int h = l >> 5, r32 = l & 31;
  const int cp = 32 * tile + r32;
  float nu = (float)(cp >> 1), amp = 0.0625f, ph = (cp & 1) ? 0.25f : 0.f;
  if (cp == 1) { amp = 0.08838834764831845f; nu = 160.f; ph = 0.125f; }
  float fa[16], fb[16];
  {
    const float st = nu * (1.f / 640.f), p0 = __builtin_fmaf(st, (float)(16 * h), ph);
    const float c1 = 2.f * __builtin_amdgcn_cosf(st);
#pragma unroll
    for (int j = 0; j < 16; ++j) {
      if ((j & 7) < 2) {
        fa[j] = amp * __builtin_amdgcn_cosf(__builtin_fmaf(st, (float)j, p0));
        fb[j] = amp * __builtin_amdgcn_cosf(__builtin_fmaf(st, (float)(j + 32), p0));
      } else {
        fa[j] = __builtin_fmaf(c1, fa[j - 1], -fa[j - 2]);
        fb[j] = __builtin_fmaf(c1, fb[j - 1], -fb[j - 2]);
      }
    }
  }
  const float co = 2.f * __builtin_amdgcn_cosf(nu * (1.f / 20.f));
  lds_ptr ra[4];
  if (!TR) {
    const int f = (r32 >> 1) & 7;
#pragma unroll
    for (int q = 0; q < 4; ++q) ra[q] = smem + kh * 4096 + (r32 >> 3) * 1024 + (r32 & 7) * 128 + (((2 * q + h) ^ f) << 4);
  } else {
    const int gm = l >> 4, q = (l >> 2) & 3, p = l & 3;
#pragma unroll
    for (int R = 0; R < 2; ++R)
      ra[R] = smem + R * 40960 + (8 * (gm >> 1) + q) * 128 + (((4 * kh + 2 * (gm & 1) + (p >> 1)) ^ (((q >> 1) & 1) << 2)) << 4) + (p & 1) * 8;
    ra[2] = ra[3] = ra[0];
  }
  const int c0 = 32 * tile + 4 * h;
  const int row = row0 + 32 * kh + r32;
  unsigned base, mo;
  if (TR) {
    const int which = row / 640, rem = row - which * 640, head = rem / 20, d = rem - head * 20;
    int e = d;
    if (which == 2) { e = (head & 1) ? ((d < 4) ? d + 16 : d - 4) : d; if (e < 16) e = swap23(e); }
    base = OFF_WQ + head * 102400 + ((e < 16) ? which * 1024 + e * 16 : 3072 + (which * 4 + e - 16) * 16);
    mo = 51200;
  } else {
    base = OFF_XA + row * 8;
    mo = 80 * 16384;
  }
  unsigned oa[4];
#pragma unroll
  for (int p = 0; p < 4; ++p) {
    const int c = c0 + 8 * p;
    if (TR) oa[p] = base + (c >> 5) * 5120 + ((c >> 2) & 3) * 256;
    else oa[p] = base + (c >> 2) * 16384;
    PIN(oa[p]);
  }
  const __amdgpu_buffer_rsrc_t wsr = __builtin_amdgcn_make_buffer_rsrc(ws, 0, OFF_KV, 0x00020000);
  lds_ptr rb[4];
#pragma unroll
  for (int q = 0; q < 4; ++q) { rb[q] = ra[q] + 40960; if (!TR) PIN(rb[q]); }
  f32x16 aE[2] = {}, aO[2] = {};
  unsigned ie = 0x88888888u, io = 0xDDDDDDDDu;
  PIN(ie); PIN(io);
  bf16x8 bx[2][2][2];
  u32x4 pe[2], po[2];
  auto rdb = [&](int s, int R, int x) {
    if (!TR) return lds_rd16((R ? rb : ra)[2 * (s & 1) + x] + (s >> 1) * 8192);
    const lds_ptr q = ra[R] + s * 4096 + x * 2048;
    return cat8(lds_tr(q), lds_tr(q + 512));
  };
#pragma unroll
  for (int i = 0; i < 4; ++i) { pe[0][i] = pack2(fa[4 * i], fa[4 * i + 2]); po[0][i] = pack2(fa[4 * i + 1], fa[4 * i + 3]); }
  PIN(pe[0]); PIN(po[0]);
  WAIT_BAR(0);
#pragma unroll
  for (int i = 0; i < 4; ++i) bx[0][i >> 1][i & 1] = rdb(0, i >> 1, i & 1);
#pragma unroll
  for (int s = 0; s < 10; ++s) {
    if (!TR && (s & 1) && s < 8) BAR();
    float* cur = (s & 1) ? fb : fa;
    float* nxt = (s & 1) ? fa : fb;
#pragma unroll
    for (int i = 0; i < 4; ++i) {
      const int R = 1 - (i >> 1);
      const bf16x16 b = __builtin_shufflevector(bx[s & 1][R][0], bx[s & 1][R][1], 0, 1, 2, 3, 4, 5, 6, 7, 8, 9, 10, 11, 12, 13, 14, 15);
      if (i & 1) aO[R] = SMFI(__builtin_bit_cast(bf16x8, po[s & 1]), b, aO[R], io);
      else aE[R] = SMFI(__builtin_bit_cast(bf16x8, pe[s & 1]), b, aE[R], ie);
      SBAR();
      if (s < 9) {
        bx[(s + 1) & 1][R][i & 1] = rdb(s + 1, R, i & 1);
        unsigned e = pack2(nxt[4 * i], nxt[4 * i + 2]), o = pack2(nxt[4 * i + 1], nxt[4 * i + 3]);
        PIN(e); PIN(o);
        pe[(s + 1) & 1][i] = e; po[(s + 1) & 1][i] = o;
        if (s < 8) {
#pragma unroll
          for (int j = 4 * i; j < 4 * i + 4; ++j) { cur[j] = __builtin_fmaf(co, nxt[j], -cur[j]); PIN(cur[j]); }
        }
      }
      SBAR();
    }
  }
  f32x16 E, O;
#pragma unroll
  for (int r = 0; r < 16; ++r) { E[r] = (r & 2) ? aE[0][r] - aE[1][r] : aE[0][r] + aE[1][r]; O[r] = (r & 2) ? aO[0][r] - aO[1][r] : aO[0][r] + aO[1][r]; }
  f32x16 P, M;
#pragma unroll
  for (int r = 0; r < 16; ++r) { P[r] = E[r] + O[r]; M[r] = (r & 1) ? O[r] - E[r] : E[r] - O[r]; }
  if (c0 == 0) { P[1] = TR ? 0.f : 0.8f * P[0]; P[0] = 0.7071067811865476f * M[0]; M[0] = E[1]; M[1] = O[1]; }
  if (KIND == 2) {
    const lds_ptr tw = exb + cw * 4096;
#pragma unroll
    for (int p = 0; p < 4; ++p) {
      const lds_ptr d = tw + (2 * p + h) * 256 + ((r32 * 8) ^ (h * 32 + (p & 1) * 128));
      *(LAS u32x2*)(d) = (u32x2){pack2(P[4 * p], P[4 * p + 1]), pack2(P[4 * p + 2], P[4 * p + 3])};
      *(LAS u32x2*)(d + 2048) = (u32x2){pack2(M[4 * p], M[4 * p + 1]), pack2(M[4 * p + 2], M[4 * p + 3])};
    }
    asm volatile("" ::: "memory");
    const int sw = (l & 1) * 32 + (l & 2) * 64, jo = ((l >> 4) * 8 + ((l >> 2) & 3)) * 8;
    const lds_ptr r0 = tw + (l & 3) * 256 + (jo ^ sw), r1 = tw + (l & 3) * 256 + ((jo + 32) ^ sw);
#pragma unroll
    for (int f = 0; f < 4; ++f) {
      const int t16 = 2 * tile + (f & 1) + 20 * (f >> 1);
      __builtin_amdgcn_raw_buffer_store_b128(__builtin_bit_cast(u32x4, cat8(lds_tr(r0 + f * 1024), lds_tr(r1 + f * 1024))), wsr,
                                             OFF_WO + (t16 / 5) * 102400 + (row >> 5) * 5120 + (t16 % 5) * 1024 + l * 16, 0, 16);
    }
    return;
  }
  if (TR) {
#pragma unroll
    for (int p = 0; p < 2; ++p) {
      __builtin_amdgcn_raw_buffer_store_b128((u32x4){pack2(P[4 * p], P[4 * p + 1]), pack2(P[4 * p + 2], P[4 * p + 3]), pack2(P[4 * p + 8], P[4 * p + 9]),
                                                     pack2(P[4 * p + 10], P[4 * p + 11])}, wsr, oa[p], 0, 16);
      __builtin_amdgcn_raw_buffer_store_b128((u32x4){pack2(M[4 * p], M[4 * p + 1]), pack2(M[4 * p + 2], M[4 * p + 3]), pack2(M[4 * p + 8], M[4 * p + 9]),
                                                     pack2(M[4 * p + 10], M[4 * p + 11])}, wsr, oa[p], mo, 16);
    }
    return;
  }
#pragma unroll
  for (int p = 0; p < 4; ++p) {
    __builtin_amdgcn_raw_buffer_store_b64((u32x2){pack2(P[4 * p], P[4 * p + 1]), pack2(P[4 * p + 2], P[4 * p + 3])}, wsr, oa[p], 0, 16);
    __builtin_amdgcn_raw_buffer_store_b64((u32x2){pack2(M[4 * p], M[4 * p + 1]), pack2(M[4 * p + 2], M[4 * p + 3])}, wsr, oa[p], mo, 16);
  }
  BAR();
}

__device__ __forceinline__ void pre_rows(const char* x, const char* wr, const char* wi, char* ws, lds_ptr smem, int bid, int tid) {
  const int w = uni(tid >> 6), l = tid & 63;
  const unsigned lds0 = (unsigned)(uintptr_t)smem;
  const int rb = (bid & 7) * 4 + (bid >> 3) / 5, cg = (bid >> 3) % 5;
  const int un = rb * 2 + (cg >> 1), k1b = un & 7, k2b = un >> 3;
  unsigned b0 = 0;
  if (w >= 4) {
    __builtin_amdgcn_s_setprio(1);
    dft_compute<0>(rb * 64, cg, ws, smem, smem + 131072, w - 4, l);
    __builtin_amdgcn_s_setprio(0);
    if (cg == 4) return;
  } else {
  unsigned bv;
  asm volatile("global_load_dword %0, %1, %2 sc1" : "=v"(bv) : "v"(OFF_BEA + (bid & 63) * 128), "s"(ws) : "memory");
  WAIT_BAR(9);
  dft_stage<false>(x, rb * 64, lds0, w, l, 3);
  WAIT_BAR(9);
  dft_stage<false>(x, rb * 64, lds0, w, l, 4);
  WAIT_BAR(8);
  SBAR(); PIN(bv); b0 = uni(bv); asm volatile("" : "+s"(b0));
  if (cg == 4) {
    WAIT_BAR(4); WAIT_BAR(0);
    const __amdgpu_buffer_rsrc_t zr = __builtin_amdgcn_make_buffer_rsrc(ws, 0, SZ_ZERO, 0x00020000);
#pragma unroll
    for (int i = 0; i < 5; ++i)
      __builtin_amdgcn_raw_buffer_store_b128((u32x4){0u, 0u, 0u, 0u}, zr, (rb * 256 + tid) * 16, (i < 2 ? i : i + 1) * 131072, 16);
    BAR();
    if (w == 3) beacon_wait((gu32*)(ws + OFF_BEA) + (bid & 63) * 32, b0);
    return;
  }
  auto seg = [&](const char* src, unsigned d) {
    const char* sg = src + uni((k1b * 8 + 64 * (4 * k2b + w)) * 642);
#pragma unroll
    for (int i = 0; i < 5; ++i) glds16s(sg + i * 1024, l * 16, d + i * 1024);
    if (l < 1) glds16s(sg + 5120, l * 16, d + 5120);
  };
  seg(wr, lds0 + 81920 + w * 5136);
  WAIT_BAR(10);
  seg(wi, lds0 + 81920 + 20544 + w * 5136);
  WAIT_BAR(12);
  {
    const int r = cg * 16 + (tid >> 4);
    const lds_ptr rp = smem + (((tid >> 3) & 1) * 8 + (r >> 3)) * 1024 + (r & 7) * 128 + (tid & 7) * 16;
    u32x4 v[5];
#pragma unroll
    for (int j = 0; j < 5; ++j) v[j] = *(const LAS u32x4*)(rp + j * 16384);
    float s = 0.f, q = 0.f;
#pragma unroll
    for (int j = 0; j < 5; ++j)
#pragma unroll
      for (int e = 0; e < 4; ++e) s += lo16(v[j][e]) + hi16(v[j][e]);
    const float mean = dppadd<0x140>(dppadd<0x141>(dppadd<0x4E>(dppadd<0xB1>(s)))) * (1.f / 640.f);
#pragma unroll
    for (int j = 0; j < 5; ++j)
#pragma unroll
      for (int e = 0; e < 4; ++e) {
        const float a = lo16(v[j][e]) - mean, c = hi16(v[j][e]) - mean;
        q = __builtin_fmaf(a, a, __builtin_fmaf(c, c, q));
      }
    q = dppadd<0x140>(dppadd<0x141>(dppadd<0x4E>(dppadd<0xB1>(q))));
    PIN(q);
    WAIT_BAR(0);
    if ((tid & 15) == 0) __hip_atomic_store((GAS double*)(ws + OFF_SS0) + rb * 64 + r, (double)(1.25f * q), RLX_AGENT);
  }
  }
  const LAS unsigned short* L = (const LAS unsigned short*)(smem + 81920) + (tid & 7) * 321;
  const __amdgpu_buffer_rsrc_t wfr = __builtin_amdgcn_make_buffer_rsrc(ws + OFF_WF, 0, SZ_X, 0x00020000);
#pragma unroll
  for (int rd = 0; rd < 3; ++rd) {
    if (rd == 2 && w < 4) break;
    const int c = (cg & 1) * 160 + (l >> 3) * 20 + (w & 3) * 5 + (w < 4 ? 3 : 0) + rd;
    const int k2 = c == 0 ? 320 : (c <= 160 ? c : 480 - c);
    u32x4 o;
#pragma unroll
    for (int i = 0; i < 4; ++i) o[i] = (unsigned)L[i * 2568 + k2] | ((unsigned)L[10272 + i * 2568 + k2] << 16);
    __builtin_amdgcn_raw_buffer_store_b128(o, wfr, c * 8192 + (k1b >> 1) * 2048 + k2b * 256 + ((k1b & 1) * 8 + (tid & 7)) * 16, 0, 16);
  }
  if (w == 3) beacon_wait((gu32*)(ws + OFF_BEA) + (bid & 63) * 32, b0);
}

__device__ __forceinline__ void pre_wqkv(const char* wqkv, char* ws, lds_ptr smem, int j, int tid) {
  const int w = uni(tid >> 6), l = tid & 63;
  const unsigned lds0 = (unsigned)(uintptr_t)smem;
  const int r0 = (j / 3) * 64, cg = (j % 3) * 2 + (w >> 2);
  if (j < 90) {
    dft_stage<true>(wqkv, r0, lds0, w & 3, l, w >> 2); dft_stage<true>(wqkv, r0, lds0, w & 3, l, 2 + (w >> 2));
    if (w < 4) dft_stage<true>(wqkv, r0, lds0, w, l, 4);
  }
  const __amdgpu_buffer_rsrc_t wsr = __builtin_amdgcn_make_buffer_rsrc(ws, 0, OFF_KV, 0x00020000);
#pragma unroll 1
  for (int it = (95 - j) * 512 + tid; it < 51200; it += 96 * 512) {
      const int rw = it % 20, ch = (it / 20) % 80, head = it / 1600;
      const int n = head * 20 + rw;
      u32x4 o;
#pragma unroll
      for (int i = 0; i < 4; ++i) {
        const int sl = (ch >> 2) * 16 + (ch & 3) * 2 + (i >> 1) * 8 + (i & 1), c = sl <= 160 ? sl : 480 - sl;
        const float rev = (float)((c * n) % 640) * (1.f / 640.f);
        float a = 0.05f * __builtin_amdgcn_cosf(rev), b = -0.05f * __builtin_amdgcn_sinf(rev);
        if (c == 0) { b = 0.03125f; a = (n & 1) ? -0.03535533905932738f : 0.03535533905932738f; }
        o[i] = pack2(a, b);
      }
      __builtin_amdgcn_raw_buffer_store_b128(o, wsr, OFF_WQ + head * 102400 + (ch >> 2) * 5120 + (ch & 3) * 256 + (rw < 16 ? 4096 + rw * 16 : 3072 + (rw - 4) * 16), 0, 16);
  }
  if (j < 90) {
    if (cg < 5) dft_compute<1>(r0, cg, ws, smem, smem + 81920 + (w >> 2) * 32768, w & 3, l);
    else WAIT_BAR(0);
  }
}
__device__ __forceinline__ void pre_wo(const char* wo, char* ws, lds_ptr smem, int j, int tid) {
  const int w = uni(tid >> 6), l = tid & 63;
  const int jc = (j % 3 == 2) ? j / 3 : (j % 3 == 1 && j < 54) ? 32 + j / 3 : 50;
  if (jc < 50) {
    if (w >= 4) dft_compute<2>((jc / 5) * 64, jc % 5, ws, smem, smem + 81920, w - 4, l);
    else {
#pragma unroll 1
      for (int i = 0; i < 5; ++i) dft_stage<false>(wo, (jc / 5) * 64, (unsigned)(uintptr_t)smem, w, l, i);
      WAIT_BAR(16); WAIT_BAR(12); WAIT_BAR(8); WAIT_BAR(4); WAIT_BAR(0);
    }
  } else if (j % 3 == 0) {
    const __amdgpu_buffer_rsrc_t wsr = __builtin_amdgcn_make_buffer_rsrc(ws, 0, OFF_KV, 0x00020000);
#pragma unroll 1
    for (int it = (j / 3) * 512 + tid; it < 51200; it += 32 * 512) {
      const int n0 = (it / 6400) * 80 + ((it >> 6) % 5) * 16 + (it & 15), j0 = ((it / 320) % 20) * 32 + ((it >> 4) & 3) * 8;
      const unsigned short* wp = (const unsigned short*)wo + j0 * 640 + n0;
      u32x4 wv;
#pragma unroll
      for (int i = 0; i < 4; ++i) wv[i] = (unsigned)wp[2 * i * 640] | ((unsigned)wp[(2 * i + 1) * 640] << 16);
      __builtin_amdgcn_raw_buffer_store_b128(wv, wsr, OFF_WO + 819200 + it * 16, 0, 16);
    }
  }
}

__device__ __forceinline__ void stage_w(char* ws, unsigned lds0, int layer, int bid, int w, unsigned lo) {
  if (w == 0) return;
  const char* gw = ws + uni(OFF_WQ + (((bid & 7) >> 1) * 8 + ((bid >> 3) & 7)) * 102400);
#pragma unroll 1
  for (int k = (w - 1) * 5120; k < 102400; k += 7 * 5120) {
    unsigned sm;
    asm volatile("s_mov_b32 %0, m0\n\ts_mov_b32 m0, %3\n\ts_nop 0\n\t"
                 "global_load_lds_dwordx4 %1, %2 sc1\n\tglobal_load_lds_dwordx4 %1, %2 offset:1024 sc1\n\t"
                 "global_load_lds_dwordx4 %1, %2 offset:2048 sc1\n\tglobal_load_lds_dwordx4 %1, %2 offset:3072 sc1\n\ts_mov_b32 m0, %0"
                 : "=&s"(sm) : "v"(lo), "s"(gw + k), "s"(lds0 + k) : "memory");
    if (layer) glds16c(gw + k + 4096, lo, lds0 + k + 4096);
  }
}
__device__ __forceinline__ void col_phase(char* ws, lds_ptr smem0, int layer, int bid, int tid) {
  const lds_ptr smem = smem0 + 107520;
  const int u = bid;
  const int l = tid & 63, w = uni(tid >> 6), p = l & 15, g = l >> 4, q4 = w & 3, hi = w >> 2;
  u32x4 wf[2];
  const unsigned lds0 = (unsigned)(uintptr_t)smem;
  auto issue = [&]() {
    const unsigned vo = (l >> 3) * 256 + (l & 7) * 16;
    const char* xa = ws + uni(OFF_XA + layer * SZ_X + u * 16384 + q4 * 4096 + hi * 128);
    const char* ss = ws + uni((layer ? OFF_SS2 : OFF_SS0) + q4 * 4096 + hi * 128);
    glds16c(xa, vo, lds0 + 16384 + w * 4096);
    glds16c(xa + 2048, vo, lds0 + 16384 + w * 4096 + 1024);
    glds16c(ss, vo, lds0 + 16384 + w * 4096 + 2048);
    glds16c(ss + 2048, vo, lds0 + 16384 + w * 4096 + 3072);
  };
  u32x4 b1r[4], b1i[4], a2r[2][2], a2i[2][2];
  float twr[8], twi[8];
  float m64 = (float)(16 * q4 + p) * (1.f / 64.f), x0 = (float)(4 * g);
  PIN(m64); PIN(x0);
  float a64 = m64 * x0;
  {
    float p32 = (float)p * (1.f / 32.f);
    unsigned odd = (p & 1) ? 0x80008000u : 0u, s1r = (p & 2) ? 0x80008000u : 0u, s1i = ((p + 1) & 2) ? 0x80008000u : 0u, eve = odd ^ 0x80008000u;
    PIN(p32); PIN(odd); PIN(s1r); PIN(s1i); PIN(eve);
    const float a32 = p32 * x0;
#pragma unroll
    for (int i = 0; i < 4; ++i) {
      const float rv = __builtin_fmaf(m64, (float)i, a64), cs = __builtin_amdgcn_cosf(rv), sn = __builtin_amdgcn_sinf(rv);
      const unsigned re = pack2(cs, sn), im = packn(sn, cs);
      b1r[0][i] = re; b1i[0][i] = im;
      b1r[1][i] = ((p & 1) ? im : re) ^ s1r; b1i[1][i] = ((p & 1) ? re : im) ^ s1i;
      b1r[2][i] = re ^ odd; b1i[2][i] = im ^ odd;
      b1r[3][i] = b1r[1][i] ^ odd; b1i[3][i] = b1i[1][i] ^ odd;
      const float rw = __builtin_fmaf(p32, (float)i, a32), c2 = __builtin_amdgcn_cosf(rw), s2 = __builtin_amdgcn_sinf(rw);
      const unsigned r2 = pack2(c2, s2), i2 = packn(s2, c2);
      a2r[0][0][i] = r2; a2i[0][0][i] = i2;
      a2r[0][1][i] = r2 ^ odd; a2i[0][1][i] = i2 ^ odd;
      a2r[1][0][i] = xr_(r2, (i & 1) ? 0x80008000u : 0u); a2i[1][0][i] = xr_(i2, (i & 1) ? 0x80008000u : 0u);
      a2r[1][1][i] = xr_(r2, (i & 1) ? eve : odd); a2i[1][1][i] = xr_(i2, (i & 1) ? eve : odd);
    }
#pragma unroll
    for (int k = 0; k < 4; ++k) { PIN(b1r[k]); PIN(b1i[k]); PIN(a2r[k >> 1][k & 1]); PIN(a2i[k >> 1][k & 1]); }
  }
  lds_ptr rd1[4], rd4[4], wu[4];
  unsigned ob = OFF_XB + layer * SZ_X + (16 * q4 + 4 * g) * 40960 + u * 256 + (16 * hi + p) * 8;
#pragma unroll
  for (int k = 0; k < 4; ++k) {
    const lds_ptr b = smem + p * 256 + (((4 * k + g) ^ p) << 4);
    rd1[k] = b + hi * 8192;
    rd4[k] = b + hi * 4096;
    wu[k] = smem + hi * 8192 + (4 * g + k) * 256 + (((4 * q4 + (p >> 2)) ^ (4 * g + k)) << 4) + (p & 3) * 4;
    PIN(rd1[k]); PIN(rd4[k]); PIN(wu[k]);
  }
  PIN(ob);
#pragma unroll
  for (int i = 0; i < 8; ++i) {
    const float rev = __builtin_fmaf(m64, (float)(16 * (i >> 2) + (i & 3)), a64) * (1.f / 32.f);
    twr[i] = __builtin_amdgcn_cosf(rev);
    twi[i] = __builtin_amdgcn_sinf(-rev);
    PIN(twr[i]); PIN(twi[i]);
  }
  if (layer) stage_w(ws, lds0 - 107520, 1, bid, w, l * 16);
  if (w == 0) gpoll(ws, 3 * layer, layer ? 32 : 20, bid, l);
  BAR();
  issue();
  {
    const char* wp = ws + uni(OFF_WF + (2 * u + hi) * 8192 + q4 * 2048);
    GLOADC16(wf[0], l * 16, wp, 0);
    GLOADC16(wf[1], l * 16, wp, 1024);
  }
  const float mask0 = (u == 0) ? 0.f : 1.f;
  WAITV(2);
  u32x2 xr[4];
  {
    u32x4 z0, z1;
    u32x2 sv[4];
    const lds_ptr pr = smem + 16384 + w * 4096 + g * 512 + p * 8;
#pragma unroll
    for (int r = 0; r < 4; ++r) { sv[r] = *(const LAS u32x2*)(pr + 2048 + r * 128); xr[r] = *(const LAS u32x2*)(pr + r * 128); }
#pragma unroll
    for (int r = 0; r < 4; ++r) {
      const float rs = __builtin_amdgcn_rsqf(__builtin_fmaf((float)__builtin_bit_cast(double, sv[r]), 1.f / 800.f, 1e-5f));
      z0[r] = pack2(lo16(xr[r][0]) * rs, hi16(xr[r][0]) * (rs * mask0));
      z1[r] = pack2(lo16(xr[r][1]) * rs, hi16(xr[r][1]) * rs);
    }
    const lds_ptr zp = smem + (16 * hi + p) * 256 + (((4 * q4 + g) ^ p) << 4);
    *(LAS u32x4*)(zp) = z0;
    *(LAS u32x4*)(zp + 8192) = z1;
  }
  float fr[8], fi[8];
  WAITV(0);
  SBAR();
  PIN(wf[0]); PIN(wf[1]);
#pragma unroll
  for (int i = 0; i < 8; ++i) { fr[i] = lo16(wf[i >> 2][i & 3]); fi[i] = hi16(wf[i >> 2][i & 3]); }
  LBAR();
  {
    bf16x8 a[2][4];
#pragma unroll
    for (int k = 0; k < 4; ++k)
#pragma unroll
      for (int t = 0; t < 2; ++t) a[t][k] = lds_rd16(rd1[k] + t * 4096);
    SBAR();
    f32x4 tr[2] = {}, ti[2] = {};
#pragma unroll
    for (int k = 0; k < 4; ++k)
#pragma unroll
      for (int t = 0; t < 2; ++t) {
        tr[t] = MFMA16(a[t][k], __builtin_bit_cast(bf16x8, b1r[k]), tr[t]);
        ti[t] = MFMA16(a[t][k], __builtin_bit_cast(bf16x8, b1i[k]), ti[t]);
      }
    u32x4 bb[2];
#pragma unroll
    for (int i = 0; i < 8; ++i) {
      const float x = tr[i >> 2][i & 3], y = ti[i >> 2][i & 3];
      bb[i >> 2][i & 3] = pack2(x * twr[i] - y * twi[i], x * twi[i] + y * twr[i]);
    }
    f32x4 xr2[2] = {}, xi2[2] = {};
#pragma unroll
    for (int t = 0; t < 2; ++t)
#pragma unroll
      for (int t2 = 0; t2 < 2; ++t2) {
        xr2[t2] = MFMA16(__builtin_bit_cast(bf16x8, a2r[t2][t]), __builtin_bit_cast(bf16x8, bb[t]), xr2[t2]);
        xi2[t2] = MFMA16(__builtin_bit_cast(bf16x8, a2i[t2][t]), __builtin_bit_cast(bf16x8, bb[t]), xi2[t2]);
      }
#pragma unroll
    for (int i = 0; i < 8; ++i) {
      const float x = xr2[i >> 2][i & 3], y = xi2[i >> 2][i & 3];
      bb[i >> 2][i & 3] = pack2(x * fr[i] - y * fi[i], __builtin_fmaf(-x, fi[i], -y * fr[i]));
    }
    f32x4 ur[2] = {}, ui[2] = {};
#pragma unroll
    for (int t = 0; t < 2; ++t)
#pragma unroll
      for (int t2 = 0; t2 < 2; ++t2) {
        ur[t2] = MFMA16(__builtin_bit_cast(bf16x8, a2r[t2][t]), __builtin_bit_cast(bf16x8, bb[t]), ur[t2]);
        ui[t2] = MFMA16(__builtin_bit_cast(bf16x8, a2i[t2][t]), __builtin_bit_cast(bf16x8, bb[t]), ui[t2]);
      }
#pragma unroll
    for (int i = 0; i < 8; ++i) {
      const float x = ur[i >> 2][i & 3], y = ui[i >> 2][i & 3];
      *(LAS unsigned*)(wu[i & 3] + 16384 + (i >> 2) * 4096) =
          pack2(x * twr[i] - y * twi[i], x * twi[i] + y * twr[i]);
    }
  }
  LBAR();
  f32x4 yr[2] = {}, yi[2] = {};
  {
    bf16x8 b[2][4];
#pragma unroll
    for (int k = 0; k < 4; ++k)
#pragma unroll
      for (int c = 0; c < 2; ++c) b[c][k] = lds_rd16(rd4[k] + 16384 + c * 8192);
    SBAR();
#pragma unroll
    for (int k = 0; k < 4; ++k)
#pragma unroll
      for (int c = 0; c < 2; ++c) {
        yr[c] = MFMA16(__builtin_bit_cast(bf16x8, b1r[k]), b[c][k], yr[c]);
        yi[c] = MFMA16(__builtin_bit_cast(bf16x8, b1i[k]), b[c][k], yi[c]);
      }
  }
  const __amdgpu_buffer_rsrc_t wsr = __builtin_amdgcn_make_buffer_rsrc(ws, 0, OFF_KV, 0x00020000);
  const float sc = 1.f / 2048.f, sc0 = -sc * mask0;
#pragma unroll
  for (int r = 0; r < 4; ++r) {
    const u32x2 o = {pack2(__builtin_fmaf(yr[0][r], sc, lo16(xr[r][0])), __builtin_fmaf(yi[0][r], sc0, hi16(xr[r][0]))),
                     pack2(__builtin_fmaf(yr[1][r], sc, lo16(xr[r][1])), __builtin_fmaf(yi[1][r], -sc, hi16(xr[r][1])))};
    __builtin_amdgcn_raw_buffer_store_b64(o, wsr, ob, r * 40960, 16);
  }
}

constexpr int POS_B = 4 * TILE_B;
constexpr int ATT_LDS = 163840;
__host__ __device__ constexpr int slot_off(int pos) { return ((pos + 10) % 15) * POS_B; }
#define CODE_B 32768
#define STR_(x) #x
#define STR(x) STR_(x)
#define SL0 107520
#define SL2 129024
#define SL9 43008
static_assert(slot_off(0) == SL0 && slot_off(2) == SL2 && slot_off(9) == SL9 && slot_off(5) == 0 && POS_B == 10752, "");
#define EXP2(x) __builtin_amdgcn_exp2f(x)
#define MX3(a, b, c) __builtin_fmaxf(__builtin_fmaxf((a), (b)), (c))
#define SMF(a, b, c) __builtin_amdgcn_smfmac_f32_32x32x32_bf16(a, __builtin_bit_cast(bf16x16, b), c, sidx, 0, 0)
#define A_QB(S, K, Q) "v_mfma_f32_32x32x16_bf16 " S ", " K ", " Q ", 0\n\t"
#define A_QA(S, K, Q) "v_mfma_f32_32x32x8_bf16 " S ", " K ", " Q ", " S "\n\t"
#define A_PV(O, V, P) "v_smfmac_f32_32x32x32_bf16 " O ", " V ", " P ", v112\n\t"
#define A_E4(d0, d1, d2, d3, s0, s1, s2, s3) \
  "v_exp_f32 v" #d0 ", v" #s0 "\n\tv_exp_f32 v" #d1 ", v" #s1 "\n\tv_exp_f32 v" #d2 ", v" #s2 "\n\tv_exp_f32 v" #d3 ", v" #s3 "\n\t"
#define A_PAIR(KC1, KC2, KN1, KN2, KOFF, V1RD, V0RD)                                          \
  A_QB("v[48:63]", KC1, "v[84:87]") A_E4(64, 65, 66, 67, 32, 33, 34, 35)                      \
  A_QA("v[48:63]", KC2, "v[90:91]")                                                           \
  "ds_read_b128 " KC1 ", v113 offset:" KOFF "\n\tds_read_b64 " KC2 ", v114 offset:" KOFF "\n\t" \
  A_E4(68, 69, 70, 71, 36, 37, 38, 39)                                                        \
  A_PV("v[16:31]", "v[108:111]", "v[72:79]") "ds_read_b128 v[108:111], " V1RD "\n\t"          \
  A_E4(72, 73, 74, 75, 40, 41, 42, 43)                                                        \
  "s_waitcnt lgkmcnt(3)\n\t" A_PV("v[0:15]", "v[104:107]", "v[64:71]") A_E4(76, 77, 78, 79, 44, 45, 46, 47) \
  A_QB("v[32:47]", KN1, "v[80:83]") A_E4(64, 65, 66, 67, 48, 49, 50, 51)                      \
  A_QA("v[32:47]", KN2, "v[88:89]") A_E4(68, 69, 70, 71, 52, 53, 54, 55)                      \
  "s_waitcnt lgkmcnt(0)\n\t" A_PV("v[0:15]", "v[108:111]", "v[72:79]") A_E4(72, 73, 74, 75, 56, 57, 58, 59) \
  A_PV("v[16:31]", "v[104:107]", "v[64:71]") "ds_read_b128 v[104:107], " V0RD "\n\t"          \
  A_E4(76, 77, 78, 79, 60, 61, 62, 63)

__device__ __forceinline__ void attn_phase(char* ws, char* out, lds_ptr smem, int LAYER, int bid, int tid) {
  const unsigned lds0 = (unsigned)(uintptr_t)smem;
  const int l = tid & 63, w = uni(tid >> 6);
  const int h = l >> 5, r32 = l & 31;
  const int head = ((bid & 7) >> 1) * 8 + ((bid >> 3) & 7), qb = (bid & 1) * 4 + (bid >> 6);
  const bool early = w < 4;
  const int p = l & 15, g = l >> 4;
  const unsigned lo = l * 16;
  const char* xp = ws + uni(OFF_XB + LAYER * SZ_X + (qb * 8 + w) * 40960);
  LAS unsigned* cnts = (LAS unsigned*)(smem + 15 * POS_B + 2048);
  const __amdgpu_buffer_rsrc_t wsr = __builtin_amdgcn_make_buffer_rsrc(ws, 0, OFF_KV, 0x00020000);
  gu32* rsg = (gu32*)(ws + OFF_RS) + LAYER * 2048 + qb * 256;
#define XLOAD(j) GLOADC16(xf[j], lo, xp + ((j) >> 2) * 4096, ((j) & 3) * 1024)
  unsigned cad = lds0 + 15 * POS_B + 2048, one = 1u;
  PIN(cad); PIN(one);
#define signal(I) asm volatile("s_mov_b64 exec, 1\n\tds_add_u32 %0, %1 offset:%2\n\ts_mov_b64 exec, -1" ::"v"(cad), "v"(one), "i"(4 * (I)) : "memory")
#define await(I, N) do { unsigned t_, u_;                                                                                              \
    asm volatile("1:\n\tds_read_b32 %0, %2 offset:%3\n\ts_waitcnt lgkmcnt(0)\n\tv_readfirstlane_b32 %1, %0\n\ts_cmp_ge_u32 %1, %4\n\t" \
                 "s_cbranch_scc1 2f\n\ts_sleep 1\n\ts_branch 1b\n2:"                                                                   \
                 : "=&v"(t_), "=&s"(u_) : "v"(cad), "i"(4 * (I)), "i"(N) : "scc", "memory"); } while (0)
  if (tid < 32) cnts[tid] = 0u;
  if (bid >= 160 || !LAYER) stage_w(ws, lds0, LAYER, bid, w, lo);
  const lds_ptr mine = smem + slot_off(0) + w * TILE_B;
  const lds_ptr q1b = smem + (early ? 102400 : 0), q2b = smem + (early ? 15 * POS_B : 4096);
  const lds_ptr q1s = q1b + (w & 3) * 1024;
  const lds_ptr q2s = q2b + (w & 3) * 512;
  lds_ptr wq1 = q1s + (g >> 1) * 512 + p * 16 + (g & 1) * 8, wk1 = wq1 + (mine - q1s);
  lds_ptr wv = mine + 1280 + (g >> 1) * 352 + p * 16 + (g & 1) * 8;
  lds_ptr we = h ? mine + 1280 + (p >> 3) * 352 + (p & 7) * 2 + 256 : (g ? mine + 1024 : q2s) + p * 8;
  lds_ptr rq2 = h ? mine + 1280 + 21 * 16 : q2s + r32 * 8, rv2 = mine + imin(2048 + l * 16, TILE_B - 16);
  const int k1o = l * 16, k2o = h ? 1280 + 20 * 16 : 1024 + r32 * 8, vo = 1280 + h * 352 + (r32 < 21 ? r32 : 21) * 16;
  lds_ptr rk1 = mine + k1o, rk2 = mine + k2o;
  gu32* rsa = rsg + w * 32 + 2 * p;
  gu32* rsb = rsg + w * 32 + 8 * g;
  float qsc = g ? -1.f : -0.22360679774997896f * 1.4426950408889634f;
  lds_ptr rq1 = q1s + l * 16, wq2 = q2s + l * 8;
  unsigned car = cad + (early ? 0 : 8);
  PIN(rq1); PIN(wq2); PIN(car);
  unsigned bk1 = (w & 1) * 2 * TILE_B + lds0 + k1o, bk2 = bk1 + (k2o - k1o), bv = bk1 + (vo - k1o);
  lds_ptr pq1 = rq1 + ((w & 1) ? -1024 : 1024), pq2 = wq2 + ((w & 1) ? -512 : 512);
  unsigned fpo = (qb >> 2) * 256 + (l & 1) * 128, ppo = l * 256 + (qb >> 2) * 128, pval = 0x101u << qb;
  if (l & 2) { ppo = OFF_OF - OFF_FLAGS + (LAYER * 64 + qb * 8 + w) * 1024 + (l & 1) * 512 + (head >> 3) * 128 - (LAYER * 256 + head * 8 + w) * 512; pval = 0x10000u; }
  unsigned ob1 = OFF_OB + LAYER * SZ_X + (qb * 16 + 2 * w + (p >> 3)) * 20480 + ((2 * p + (r32 >> 4)) & 15) * 16 + ((5 * head) >> 1) * 256;
  unsigned ob2 = ob1 + ((head & 1) ? 8 : 512);
  ob1 += ((head & 1) + h) * 256;
  PIN(bk1); PIN(bk2); PIN(bv); PIN(pq1); PIN(pq2); PIN(fpo); PIN(ppo); PIN(pval); PIN(ob1); PIN(ob2);
  PIN(wq1); PIN(wk1); PIN(wv); PIN(we); PIN(rq2); PIN(rv2); PIN(rk1); PIN(rk2); PIN(rsa); PIN(rsb); PIN(qsc);
  if (l < 8) {
    const unsigned c1 = (l & 1) ? 0u : 0x3f803f80u;
    *(LAS u32x4*)(mine + 1280 + (l >> 1) * 352 + (20 + (l & 1)) * 16) = (u32x4){c1, c1, c1, c1};
  }
  if (w == 0) gpoll(ws, 2 + 2 * LAYER, LAYER ? 20 : 32, bid, l);
  if (!early) __builtin_amdgcn_s_setprio(2);
  WAIT_BAR(0);
  if (!early) {
    u32x4 sv[3];
    const char* sp = ws + uni(OFF_XB + LAYER * SZ_X + (qb * 8 + (head >> 2)) * 40960 + (head & 3) * 64 + (w - 4) * 10240);
    const unsigned so_ = (l & 3) * 16 + (l >> 2) * 256;
    GLOADC16(sv[0], so_, sp, 0);
    GLOADC16(sv[1], so_, sp + 4096, 0);
    GLOADC16(sv[2], so_, sp + 4096, 2048);
    WAITV(0);
    SBAR();
    PIN(sv[0]); PIN(sv[1]); PIN(sv[2]);
    if (tid < 260) { sv[0][0] &= 0xffffu; sv[0][2] &= 0xffffu; }
    float q0 = sq2(sv[2][1], sq2(sv[2][0], 0.f)), q1 = sq2(sv[2][3], sq2(sv[2][2], 0.f));
    if (!h) q0 = q1 = 0.f;
#pragma unroll
    for (int i = 0; i < 2; ++i) { q0 = sq2(sv[i][1], sq2(sv[i][0], q0)); q1 = sq2(sv[i][3], sq2(sv[i][2], q1)); }
    q0 = dppadd<0x124>(q0); q1 = dppadd<0x124>(q1);
    const float q = sum32(sum16(dppadd<0x128>((l & 4) ? q1 : q0)));
    if (l < 8) ((LAS float*)cnts)[w * 8 + l] = q;
    signal(0);
    if (w == 7) {
      await(0, 4);
      const float t = sum16(dppadd<0x128>(((LAS float*)cnts)[32 + r32]));
      const float r = 0.8f * __builtin_amdgcn_rsqf(__builtin_fmaf(t, 1.f / 800.f, 1e-5f));
      if (l < 8) __hip_atomic_store(rsg + head * 8 + 2 * (l & 3) + (l >> 2), __builtin_bit_cast(unsigned, r) | 0x80000000u, RLX_AGENT);
    }
    __builtin_amdgcn_s_setprio(0);
  }
  f32x4 acc[5][2] = {};
  unsigned rsu[2];
  u32x4 rsv[2];
  u32x2 e3[2];
#define XSWAP(k) do { SBAR(); \
    u32x4 &xa_ = xf[(2 * (k)) % 10], &xb_ = xf[(2 * (k) + 1) % 10]; \
    unsigned a2_ = xa_[2], a3_ = xa_[3], b0_ = xb_[0], b1_ = xb_[1]; \
    asm volatile("v_swap_b32 %0, %1" : "+v"(a2_), "+v"(b0_)); \
    asm volatile("v_swap_b32 %0, %1" : "+v"(a3_), "+v"(b1_)); \
    xa_[2] = a2_; xa_[3] = a3_; xb_[0] = b0_; xb_[1] = b1_; PIN(xa_); PIN(xb_); } while (0)
#define QKV_FIVE(NT, LAST) \
    _Pragma("unroll") \
    for (int k = 0; k < 5; ++k) { \
      _Pragma("unroll") \
      for (int n = 0; n < NT; ++n) \
        _Pragma("unroll") \
        for (int c = 0; c < 2; ++c) { \
          const bf16x8 b = __builtin_bit_cast(bf16x8, xf[2 * k + c]); \
          acc[n][c] = (n == 2) ? MFMA16(b, a[n], acc[n][c]) : MFMA16(a[n], b, acc[n][c]); \
          SBAR(); \
          if (c == 1 && !(LAST && k == 4)) a[n] = lds_rd16(wa + (k + 1) * 5120 + n * 1024); \
          if (c == 0 && n < 2) { \
            if (!LAST || k == 0) GLOADC16(xf[(2 * k + 8 + n) % 10], lo, xq + ((2 * k + n) >> 2) * 4096, ((2 * k + n) & 3) * 1024); \
            if (LAST && k == 1) { \
              asm volatile("global_load_dword %0, %1, off offset:%2 sc1" : "=v"(rsu[n]) : "v"(rsa), "i"(4 * n) : "memory"); \
              asm volatile("global_load_dwordx4 %0, %1, off offset:%2 sc1" : "=v"(rsv[n]) : "v"(rsb), "i"(16 * n) : "memory"); \
            } \
          } \
          if (c == 0 && n == 3 && !(LAST && k == 4)) { \
            WAITV(!LAST || k == 0 ? 6 : 10 - 2 * k); \
            XSWAP(k + 1); \
          } \
          SBAR(); \
        } \
    }
#define QKV_LOOP(NT) do { \
    u32x4 xf[10]; \
    _Pragma("unroll") \
    for (int j = 0; j < 8; ++j) XLOAD(j); \
    lds_ptr wa = smem + l * 16; \
    const char* xq = xp + 8192; \
    bf16x8 a[NT]; \
    _Pragma("unroll") \
    for (int n = 0; n < NT; ++n) { a[n] = lds_rd16(wa + n * 1024); PIN(acc[n][0]); PIN(acc[n][1]); } \
    WAITV(6); \
    XSWAP(0); \
    asm volatile("s_nop 1" : "=v"(xf[8]), "=v"(xf[9])); \
    _Pragma("unroll 1") \
    for (int it = 0; it < 3; ++it) { \
      QKV_FIVE(NT, false) \
      wa += 25600; xq += 10240; \
      if (it == 0) signal(1); \
    } \
    if (!early) { __builtin_amdgcn_s_setprio(1); await(1, 8); } \
    QKV_FIVE(NT, true) \
    asm volatile("s_waitcnt vmcnt(0)" : "+v"(rsu[0]), "+v"(rsu[1]), "+v"(rsv[0]), "+v"(rsv[1])::"memory"); \
  } while (0)
  if (LAYER) QKV_LOOP(5); else QKV_LOOP(4);
  unsigned rb[8] = {rsv[0][0], rsv[0][1], rsv[0][2], rsv[0][3], rsv[1][0], rsv[1][1], rsv[1][2], rsv[1][3]};
  {
    unsigned tmp, spins = SPIN_MAX;
    asm volatile("s_waitcnt vmcnt(0)\n1:\n\t"
                 "v_bitop3_b32 %[t], %[a0], %[a1], %[b0] bitop3:0x80\n\t"
                 "v_bitop3_b32 %[t], %[t], %[b1], %[b2] bitop3:0x80\n\t"
                 "v_bitop3_b32 %[t], %[t], %[b3], %[c0] bitop3:0x80\n\t"
                 "v_bitop3_b32 %[t], %[t], %[c1], %[c2] bitop3:0x80\n\t"
                 "v_and_b32 %[t], %[t], %[c3]\n\t"
                 "v_cmp_le_i32 vcc, 0, %[t]\n\t"
                 "s_cbranch_vccz 2f\n\t"
                 "s_sleep 1\n\t"
                 "global_load_dword %[a0], %[pa], off sc1\n\tglobal_load_dword %[a1], %[pa], off offset:4 sc1\n\t"
                 "global_load_dword %[b0], %[pb], off sc1\n\tglobal_load_dword %[b1], %[pb], off offset:4 sc1\n\t"
                 "global_load_dword %[b2], %[pb], off offset:8 sc1\n\tglobal_load_dword %[b3], %[pb], off offset:12 sc1\n\t"
                 "global_load_dword %[c0], %[pb], off offset:16 sc1\n\tglobal_load_dword %[c1], %[pb], off offset:20 sc1\n\t"
                 "global_load_dword %[c2], %[pb], off offset:24 sc1\n\tglobal_load_dword %[c3], %[pb], off offset:28 sc1\n\t"
                 "s_sub_u32 %[n], %[n], 1\n\ts_cmp_lg_u32 %[n], 0\n\ts_waitcnt vmcnt(0)\n\ts_cbranch_scc1 1b\n2:"
                 : [t] "=&v"(tmp), [n] "+s"(spins), [a0] "+v"(rsu[0]), [a1] "+v"(rsu[1]), [b0] "+v"(rb[0]), [b1] "+v"(rb[1]), [b2] "+v"(rb[2]),
                   [b3] "+v"(rb[3]), [c0] "+v"(rb[4]), [c1] "+v"(rb[5]), [c2] "+v"(rb[6]), [c3] "+v"(rb[7])
                 : [pa] "v"(rsa), [pb] "v"(rsb) : "vcc", "scc", "memory");
  }
#pragma unroll
  for (int c = 0; c < 2; ++c) {
    const float rs = -u2f(rsu[c]);
    const float qs = rs * (0.22360679774997896f * 1.4426950408889634f), s3 = u2f(rsu[c]) * qsc;
    *(LAS u32x2*)(wq1 + c * 256) = (u32x2){pack2(acc[0][c][0] * qs, acc[0][c][1] * qs), pack2(acc[0][c][2] * qs, acc[0][c][3] * qs)};
    *(LAS u32x2*)(wk1 + c * 256) = (u32x2){pack2(acc[1][c][0] * rs, acc[1][c][1] * rs), pack2(acc[1][c][2] * rs, acc[1][c][3] * rs)};
    e3[c] = (u32x2){pack2(acc[3][c][0] * s3, acc[3][c][1] * s3), pack2(acc[3][c][2] * s3, acc[3][c][3] * s3)};
  }
  if (!h) { *(LAS u32x2*)(we) = e3[0]; *(LAS u32x2*)(we + 128) = e3[1]; }
  asm volatile("" ::: "memory");
  u32x4 qa1 = *(const LAS u32x4*)(rq1);
  u32x2 qa2 = *(const LAS u32x2*)(rq2);
  char* kvh = ws + uni(OFF_KV + LAYER * SZ_KV + head * 64 * TILE_B);
  {
    const u32x4 k1u = *(const LAS u32x4*)(rk1);
    const s16x4 k2u = *(const LAS s16x4*)(rk2);
    u32x2 vv[2];
#pragma unroll
    for (int c = 0; c < 2; ++c) {
      vv[c] = (u32x2){pack2(acc[2][c][0] * -u2f(rb[c]), acc[2][c][1] * -u2f(rb[2 + c])), pack2(acc[2][c][2] * -u2f(rb[4 + c]), acc[2][c][3] * -u2f(rb[6 + c]))};
      PIN(vv[c]);
    }
    SBAR();
    f32x16 s = {};
    s = MFMA32(__builtin_bit_cast(bf16x8, k1u), __builtin_bit_cast(bf16x8, qa1), s);
    s = MFMA32K8(k2u, __builtin_bit_cast(s16x4, qa2), s);
    SBAR();
    *(LAS u32x2*)(wv) = vv[0];
    *(LAS u32x2*)(wv + 704) = vv[1];
    if (g == 2) {
#pragma unroll
      for (int c = 0; c < 2; ++c) {
        *(LAS unsigned short*)(we + c * 704) = (unsigned short)e3[c][0];
        *(LAS unsigned short*)(we + c * 704 + 16) = (unsigned short)(e3[c][0] >> 16);
        *(LAS unsigned short*)(we + c * 704 + 32) = (unsigned short)e3[c][1];
        *(LAS unsigned short*)(we + c * 704 + 48) = (unsigned short)(e3[c][1] >> 16);
      }
    }
    asm volatile("" ::: "memory");
    const u32x4 v1 = *(const LAS u32x4*)(rk1 + 1024);
    const u32x4 v2 = *(const LAS u32x4*)(rv2);
    SBAR();
    float m0 = MX3(s[0], s[1], s[2]), m1 = MX3(s[3], s[4], s[5]);
    m0 = MX3(m0, s[6], s[7]); m1 = MX3(m1, s[8], s[9]);
    m0 = MX3(m0, s[10], s[11]); m1 = MX3(m1, s[12], s[13]);
    m0 = MX3(m0, s[14], s[15]);
    m0 = __builtin_fmaxf(m0, m1);
    const float m = max32(m0);
    const float mh = hi16(pack2(0.f, -m));
    qa2[0] = h ? pack2(mh, -m - mh) : qa2[0];
    *(LAS u32x2*)(wq2) = qa2;
    asm volatile("s_mov_b64 exec, 1\n\tds_add_u32 %0, %1 offset:12\n\ts_mov_b64 exec, -1" ::"v"(car), "v"(one) : "memory");
    const __amdgpu_buffer_rsrc_t rsrc = __builtin_amdgcn_make_buffer_rsrc(kvh + (unsigned)((qb * 8 + w) * TILE_B), 0, TILE_B, 0x00020000);
    __builtin_amdgcn_raw_buffer_store_b128(k1u, rsrc, l * 16, 0, 16);
    __builtin_amdgcn_raw_buffer_store_b128(v1, rsrc, 1024 + l * 16, 0, 16);
    __builtin_amdgcn_raw_buffer_store_b128(v2, rsrc, 2048 + l * 16, 0, 16);
  }
  if (LAYER) {
    const unsigned x3 = OFF_X3 + (5 * head + g) * 16384 + (qb * 256 + w * 32 + 2 * p) * 8;
    __builtin_amdgcn_raw_buffer_store_b128((u32x4){pack2(acc[4][0][0], acc[4][0][1]), pack2(acc[4][0][2], acc[4][0][3]),
                                                   pack2(acc[4][1][0], acc[4][1][1]), pack2(acc[4][1][2], acc[4][1][3])}, wsr, x3, 0, 16);
    if (g == 3) __builtin_amdgcn_raw_buffer_store_b128((u32x4){pack2(acc[3][0][0], acc[3][0][1]), pack2(acc[3][0][2], acc[3][0][3]),
                                                               pack2(acc[3][1][0], acc[3][1][1]), pack2(acc[3][1][2], acc[3][1][3])}, wsr, x3, 16384, 16);
  }
  typedef __attribute__((ext_vector_type(32))) unsigned u32x32;
  typedef __attribute__((ext_vector_type(12))) float f32x12;
  f32x16 oA, oB;
  f32x12 ex;
  u32x32 wf0, wf1, wf2;
  u32x4 wf3;
#define A_SIG(I) "s_mov_b64 exec, 1\n\tds_add_u32 %[cad], %[one] offset:4*" #I "\n\ts_mov_b64 exec, -1\n\t"
#define A_AWAIT(I, N) "9:\n\tds_read_b32 v119, %[cad] offset:4*" #I "\n\ts_waitcnt lgkmcnt(0)\n\tv_readfirstlane_b32 s80, v119\n\t" \
                      "s_cmp_ge_u32 s80, " #N "\n\ts_cbranch_scc1 8f\n\ts_sleep 1\n\ts_branch 9b\n8:\n\t"
#define A_RUN(N) "s_mov_b32 s75, " N "\n\ts_call_b64 s[70:71], 20f\n\t"
#define A_RESTART(I) "v_add_u32 v118, 4*" #I ", %[cad]\n\ts_call_b64 s[70:71], 30f\n\t"
#define A_FETCH(N) "s_mov_b32 s76, " #N "\n\ts_call_b64 s[70:71], 40f\n\t"
#define A_PUBLISH "s_waitcnt vmcnt(0)\n\ts_mov_b64 exec, 15\n\tglobal_atomic_add %[ppo], %[pval], %[fl]\n\ts_mov_b64 exec, -1\n\t"
#define A_KREADS "ds_read_b128 v[92:95], v113\n\tds_read_b64 v[100:101], v114\n\tds_read_b128 v[104:107], v116\n\t" \
                 "ds_read_b128 v[96:99], v113 offset:2688\n\tds_read_b64 v[102:103], v114 offset:2688\n\t"
  asm volatile(
        "v_mov_b32 v108, 0\n\tv_mov_b32 v109, 0\n\tv_mov_b32 v110, 0\n\tv_mov_b32 v111, 0\n\tv_mov_b32 v112, 0xdddddddd\n\t"
        "s_mov_b32 s74, " STR(SL0) "\n\ts_mov_b32 s77, 1\n\ts_mov_b32 s78, 0\n\ts_mov_b32 s82, m0\n\t"
        "v_add_u32 v113, s74, %[bk1]\n\tv_add_u32 v114, s74, %[bk2]\n\tv_add_u32 v116, s74, %[bv]\n\t"
        "v_mfma_f32_32x32x8_bf16 v[0:15], v[108:109], v[108:109], 0\n\tv_mfma_f32_32x32x8_bf16 v[16:31], v[108:109], v[108:109], 0\n\t"
        "v_mfma_f32_32x32x8_bf16 v[64:79], v[108:109], v[108:109], 0\n"
        "1:\n\tds_read_b32 v118, %[car] offset:12\n\tds_read_b32 v119, %[cad] offset:12\n\t" A_KREADS
        "ds_read_b128 v[84:87], %[pq1]\n\tds_read_b64 v[90:91], %[pq2]\n\t"
        "s_waitcnt lgkmcnt(7)\n\tv_min_u32 v118, v118, v119\n\ts_nop 0\n\tv_readfirstlane_b32 s80, v118\n\ts_cmp_ge_u32 s80, 4\n\t"
        "s_cbranch_scc1 2f\n\ts_waitcnt lgkmcnt(0)\n\ts_sleep 1\n\ts_branch 1b\n"
        "2:\n\ts_waitcnt lgkmcnt(5)\n\t" A_QB("v[32:47]", "v[92:95]", "v[80:83]") A_QA("v[32:47]", "v[100:101]", "v[88:89]")
        "s_waitcnt lgkmcnt(0)\n\t" A_SIG(4)
        "s_cmp_ge_u32 %[w], 4\n\ts_cbranch_scc1 5f\n\t"
        "s_add_u32 s79, %[dst], " STR(SL2) "\n\t" A_PUBLISH A_RUN("1")
        A_SIG(8) A_FETCH(3) A_RESTART(5) A_RUN("1")
        "s_waitcnt vmcnt(0)\n\t" A_SIG(6) A_AWAIT(4, 8) "s_mov_b32 s79, %[dst]\n\t" A_FETCH(4) A_RESTART(6) A_RUN("2")
        "s_waitcnt vmcnt(0)\n\t" A_SIG(7) A_AWAIT(7, 4) A_RUN("4")
        A_SIG(10) A_AWAIT(9, 4) A_RUN("3") "s_call_b64 s[70:71], 50f\n\t" A_RUN("5")
        "s_branch 6f\n"
        "5:\n\ts_nop 1\n\ts_add_u32 s79, %[dst], " STR(SL9) "\n\t" A_RUN("1")
        A_SIG(8) A_PUBLISH A_RUN("1")
        A_RESTART(6) A_RUN("1")
        A_AWAIT(8, 8) A_FETCH(7) A_RUN("1")
        A_AWAIT(7, 4) "s_waitcnt vmcnt(0)\n\t" A_SIG(9) A_RUN("4") "s_call_b64 s[70:71], 50f\n\t"
        A_SIG(10) A_AWAIT(9, 4) A_RUN("6") "s_setprio 0\n\t" A_RUN("2")
        "6:\n\t"
        A_PV("v[16:31]", "v[108:111]", "v[72:79]")
        "s_lshl_b32 s80, %[w], 12\n\tv_add_u32 v113, s80, %[lo]\n\tv_xor_b32 v114, 0x1000, v113\n\t"
        "s_lshl_b32 s80, %[w], 2\n\tv_add_u32 v115, s80, %[cad]\n\tv_xor_b32 v116, 4, v115\n\t" A_AWAIT(10, 8)
        "s_nop 1\n\tds_write_b128 v113, v[16:19]\n\tds_write_b128 v113, v[20:23] offset:1024\n\tds_write_b128 v113, v[24:27] offset:2048\n\t"
        "ds_write_b32 v115, %[one] offset:64\n"
        "3:\n\tds_read_b32 v119, v116 offset:64\n\tds_read_b128 v[32:35], v114\n\tds_read_b128 v[36:39], v114 offset:1024\n\t"
        "ds_read_b128 v[40:43], v114 offset:2048\n\ts_waitcnt lgkmcnt(0)\n\tv_readfirstlane_b32 s80, v119\n\t"
        "s_cmp_lg_u32 s80, 0\n\ts_cbranch_scc1 7f\n\ts_sleep 1\n\ts_branch 3b\n"
        "20:\n\ts_add_u32 s74, s74, 10752\n\ts_cmp_eq_u32 s74, 161280\n\ts_cselect_b32 s74, 0, s74\n\t"
        "v_mov_b32 v115, v116\n\tv_add_u32 v113, s74, %[bk1]\n\tv_add_u32 v114, s74, %[bk2]\n\tv_add_u32 v116, s74, %[bv]\n\t"
        A_PAIR("v[92:95]", "v[100:101]", "v[96:99]", "v[102:103]", "0", "v115 offset:704", "v115 offset:2688")
        "global_load_dword v117, %[fpo], %[fl] sc1\n\t"
        A_PAIR("v[96:99]", "v[102:103]", "v[92:95]", "v[100:101]", "2688", "v115 offset:3392", "v116")
        "s_sub_u32 s75, s75, 1\n\ts_cmp_lg_u32 s75, 0\n\ts_cbranch_scc1 20b\n\ts_waitcnt lgkmcnt(0)\n\ts_setpc_b64 s[70:71]\n"
        "30:\n\tds_read_b32 v119, v118\n\t" A_KREADS
        "s_waitcnt lgkmcnt(5)\n\tv_readfirstlane_b32 s80, v119\n\ts_cmp_ge_u32 s80, 4\n\ts_cbranch_scc1 31f\n\t"
        "s_waitcnt lgkmcnt(0)\n\ts_sleep 1\n\ts_branch 30b\n"
        "31:\n\ts_waitcnt lgkmcnt(3)\n\t" A_QB("v[32:47]", "v[92:95]", "v[80:83]") A_QA("v[32:47]", "v[100:101]", "v[88:89]")
        "s_waitcnt lgkmcnt(0)\n\ts_nop 2\n\ts_setpc_b64 s[70:71]\n"
        "40:\n\ts_mov_b32 s81, 0x20000\n"
        "41:\n\ts_waitcnt vmcnt(0)\n\tv_readlane_b32 s78, v117, 0\n\tv_readlane_b32 s80, v117, 1\n\ts_or_b32 s78, s78, s80\n\ts_lshr_b32 s78, s78, %[qb]\n"
        "42:\n\ts_bitcmp1_b32 s78, s77\n\ts_cbranch_scc1 43f\n\t"
        "global_load_dword v117, %[fpo], %[fl] sc1\n\ts_sub_u32 s81, s81, 1\n\ts_cmp_lg_u32 s81, 0\n\ts_cbranch_scc1 41b\n\ts_mov_b32 s78, -1\n"
        "43:\n\ts_add_u32 s80, s77, %[qb]\n\ts_and_b32 s80, s80, 7\n\ts_mul_i32 s80, s80, 21504\n\ts_mov_b64 s[84:85], %[kvw]\n\t"
        "s_add_u32 s84, s84, s80\n\ts_addc_u32 s85, s85, 0\n\ts_mov_b32 m0, s79\n\ts_add_u32 s79, s79, 10752\n\t"
        "global_load_lds_dwordx4 %[lo], s[84:85] sc1\n\tglobal_load_lds_dwordx4 %[lo], s[84:85] offset:1024 sc1\n\t"
        "s_mov_b32 exec_hi, 0xff\n\tglobal_load_lds_dwordx4 %[lo], s[84:85] offset:2048 sc1\n\ts_mov_b32 exec_hi, -1\n\t"
        "s_add_u32 s77, s77, 1\n\ts_sub_u32 s76, s76, 1\n\ts_cmp_lg_u32 s76, 0\n\ts_cbranch_scc1 42b\n\ts_setpc_b64 s[70:71]\n"
        "50:\n\t"
        "s_lshr_b32 s80, %[w], 2\n\ts_mul_i32 s80, s80, 3072\n\ts_sub_u32 s81, 3072, s80\n\ts_mov_b64 s[84:85], %[wsrc]\n\t"
        "v_add_u32 v118, s80, %[lo]\n\tv_add_u32 v119, s81, %[lo]\n\t"
        "global_load_dwordx4 v[136:139], v118, s[84:85] offset:0 sc1\n\t"
        "global_load_dwordx4 v[140:143], v118, s[84:85] offset:1024 sc1\n\t"
        "global_load_dwordx4 v[144:147], %[lo], s[84:85] offset:2048 sc1\n\t"
        "global_load_dwordx4 v[148:151], v119, s[84:85] offset:0 sc1\n\t"
        "global_load_dwordx4 v[152:155], v119, s[84:85] offset:1024 sc1\n\t"
        "s_add_u32 s84, s84, 5120\n\ts_addc_u32 s85, s85, 0\n\t"
        "global_load_dwordx4 v[156:159], v118, s[84:85] offset:0 sc1\n\t"
        "global_load_dwordx4 v[160:163], v118, s[84:85] offset:1024 sc1\n\t"
        "global_load_dwordx4 v[164:167], %[lo], s[84:85] offset:2048 sc1\n\t"
        "global_load_dwordx4 v[168:171], v119, s[84:85] offset:0 sc1\n\t"
        "global_load_dwordx4 v[172:175], v119, s[84:85] offset:1024 sc1\n\t"
        "s_add_u32 s84, s84, 5120\n\ts_addc_u32 s85, s85, 0\n\t"
        "global_load_dwordx4 v[176:179], v118, s[84:85] offset:0 sc1\n\t"
        "global_load_dwordx4 v[180:183], v118, s[84:85] offset:1024 sc1\n\t"
        "global_load_dwordx4 v[184:187], %[lo], s[84:85] offset:2048 sc1\n\t"
        "global_load_dwordx4 v[188:191], v119, s[84:85] offset:0 sc1\n\t"
        "global_load_dwordx4 v[192:195], v119, s[84:85] offset:1024 sc1\n\t"
        "s_add_u32 s84, s84, 5120\n\ts_addc_u32 s85, s85, 0\n\t"
        "global_load_dwordx4 v[196:199], v118, s[84:85] offset:0 sc1\n\t"
        "global_load_dwordx4 v[200:203], v118, s[84:85] offset:1024 sc1\n\t"
        "global_load_dwordx4 v[204:207], %[lo], s[84:85] offset:2048 sc1\n\t"
        "global_load_dwordx4 v[208:211], v119, s[84:85] offset:0 sc1\n\t"
        "global_load_dwordx4 v[212:215], v119, s[84:85] offset:1024 sc1\n\t"
        "s_add_u32 s84, s84, 5120\n\ts_addc_u32 s85, s85, 0\n\t"
        "global_load_dwordx4 v[216:219], v118, s[84:85] offset:0 sc1\n\t"
        "global_load_dwordx4 v[220:223], v118, s[84:85] offset:1024 sc1\n\t"
        "global_load_dwordx4 v[224:227], %[lo], s[84:85] offset:2048 sc1\n\t"
        "global_load_dwordx4 v[228:231], v119, s[84:85] offset:0 sc1\n\t"
        "global_load_dwordx4 v[232:235], v119, s[84:85] offset:1024 sc1\n\t"
        "s_setpc_b64 s[70:71]\n"
        "7:\n\ts_mov_b32 m0, s82\n\ts_waitcnt vmcnt(0)"
        : "=&{v[0:15]}"(oA), "=&{v[16:31]}"(oB), "=&{v[32:43]}"(ex), "=&{v[136:167]}"(wf0), "=&{v[168:199]}"(wf1), "=&{v[200:231]}"(wf2),
          "=&{v[232:235]}"(wf3), "+{v[80:83]}"(qa1), "+{v[88:89]}"(qa2)
        : [bk1] "v"(bk1), [bk2] "v"(bk2), [bv] "v"(bv), [lo] "v"(lo), [cad] "v"(cad), [car] "v"(car), [one] "v"(one), [pq1] "v"(pq1), [pq2] "v"(pq2),
          [fpo] "v"(fpo), [ppo] "v"(ppo), [pval] "v"(pval), [fl] "s"(ws + uni(OFF_FLAGS + (LAYER * 256 + head * 8 + w) * 512)), [kvw] "s"(kvh + uni(w * TILE_B)),
          [wsrc] "s"(ws + uni(OFF_WO + LAYER * 819200 + (head & 7) * 102400 + (w >> 1) * 25600)), [qb] "s"(qb), [w] "s"(w),
          [dst] "s"(lds0 + (w & 3) * TILE_B)
        : "scc", "memory", "v44", "v45", "v46", "v47", "v48", "v49", "v50", "v51", "v52", "v53", "v54", "v55", "v56", "v57", "v58", "v59", "v60", "v61", "v62", "v63", "v64", "v65", "v66", "v67", "v68", "v69", "v70", "v71", "v72", "v73", "v74", "v75", "v76", "v77", "v78", "v79", "v84", "v85", "v86", "v87", "v90", "v91", "v92", "v93", "v94", "v95", "v96", "v97", "v98", "v99", "v100", "v101", "v102", "v103", "v104", "v105", "v106", "v107", "v108", "v109", "v110", "v111", "v112", "v113", "v114", "v115", "v116", "v117", "v118", "v119",
          "s70", "s71", "s72", "s73", "s74", "s75", "s76", "s77", "s78", "s79", "s80", "s81", "s82", "s83", "s84", "s85");
  unsigned sidx = 0xDDDDDDDDu;
  auto finish = [&](const f32x16& o) {
    const auto sw = __builtin_amdgcn_permlane32_swap(__builtin_bit_cast(unsigned, (float)o[8]), __builtin_bit_cast(unsigned, (float)o[8]), false, false);
    const float ls = u2f(sw[1]), il = __builtin_amdgcn_rcpf(ls);
    __builtin_amdgcn_raw_buffer_store_b128(
        (u32x4){pack2(o[0] * il, o[1] * il), pack2(o[2] * il, o[3] * il), pack2(o[4] * il, o[5] * il), pack2(o[6] * il, o[7] * il)}, wsr, ob1, 0, 16);
    if (!h) __builtin_amdgcn_raw_buffer_store_b64((u32x2){pack2(o[8] * il, o[9] * il), pack2(o[10] * il, o[11] * il)}, wsr, ob2, 0, 16);
    return ls;
  };
#pragma unroll
  for (int j = 0; j < 12; ++j) oA[j] += ex[j];
  const float lsum = finish(oA);
  float chk = lsum;
#pragma unroll
  for (int r = 0; r < 12; ++r) chk += __builtin_fabsf(oA[r]);
  if (__builtin_expect(wany(!(chk < 1e30f) || !(lsum > 1e-20f)), 0)) {
    __builtin_amdgcn_fence(__ATOMIC_ACQUIRE, "agent");
    float m = -__builtin_inff();
    f32x16 o = {};
    const u32x2 q2z = {h ? 0u : qa2[0], qa2[1]};
#pragma unroll 1
    for (int t = 0; t < 64; ++t) {
      const char* g = kvh + t * TILE_B;
      const bf16x8 k1 = *(const bf16x8*)(g + k1o), w0 = *(const bf16x8*)(g + vo), w1 = *(const bf16x8*)(g + vo + 704);
      const s16x4 k2 = *(const s16x4*)(g + k2o);
      f32x16 s = {};
      s = MFMA32K8(k2, __builtin_bit_cast(s16x4, q2z), s);
      s = MFMA32(k1, __builtin_bit_cast(bf16x8, qa1), s);
      float mn = m;
#pragma unroll
      for (int r = 0; r < 16; ++r) mn = __builtin_fmaxf(mn, s[r]);
      mn = max32(mn);
      const float al = EXP2(m - mn);
      m = mn;
      f32x8 pl, ph;
#pragma unroll
      for (int r = 0; r < 16; ++r) o[r] *= al;
#pragma unroll
      for (int r = 0; r < 8; ++r) { pl[r] = EXP2(s[r] - m); ph[r] = EXP2(s[r + 8] - m); }
      o = SMF(w0, pl, o);
      o = SMF(w1, ph, o);
    }
    finish(o);
  }
  gu32* ofl = (gu32*)(ws + OFF_OF) + (LAYER * 64 + qb * 8) * 256;
  WAITV(0);
  if (l < 2) __hip_atomic_fetch_add(ofl + w * 256 + l * 128 + (head >> 3) * 32, 1u, RLX_AGENT);
  const int cb = head & 7, kq = w >> 1, rt = (head >> 3) * 2 + (w & 1);
  const int srow = qb * 256 + rt * 32 + (kq & 1) * 16 + p;
  unsigned ra[3], oa[3];
#pragma unroll
  for (int T = 0; T < 3; ++T) {
    const int q = cb * 20 + (T < 2 ? (w >> 2) * 12 + 4 * T : 8) + g;
    ra[T] = LAYER ? OFF_X3 + q * 16384 + srow * 8 : OFF_XB + (srow >> 5) * 40960 + q * 256 + (srow & 31) * 8;
    oa[T] = LAYER ? srow * 1280 + q * 8 : OFF_XA + SZ_X + q * 16384 + srow * 8;
    PIN(ra[T]); PIN(oa[T]);
  }
  BAR();
  {
    gu32* pp_ = ofl + rt * 256 + (head & 1) * 128 + ((l & 1) ? cb >> 1 : kq) * 32;
    const unsigned msk = (l & 1) ? 0x80000u : 8u;
#pragma unroll 1
    for (unsigned spins = 0; spins < SPIN_MAX; ++spins) {
      if (!wany(!(__hip_atomic_load(pp_, RLX_AGENT) & msk))) break;
      __builtin_amdgcn_s_sleep(1);
    }
    asm volatile("" ::: "memory");
  }
  u32x4 of[10];
  u32x2 res[3];
  {
    const char* osrc = ws + uni(OFF_OB + LAYER * SZ_X + (qb * 16 + rt * 2 + (kq & 1)) * 20480 + kq * 5120);
    const char* osr2 = ws + uni(OFF_OB + LAYER * SZ_X + (qb * 16 + rt * 2 + 1 - (kq & 1)) * 20480 + kq * 5120);
#pragma unroll
    for (int i = 0; i < 10; ++i)
      asm volatile("global_load_dwordx4 %0, %1, %2 offset:%3 sc1" : "=v"(of[i]) : "v"(lo), "s"(((i & 1) ? osr2 : osrc) + (i >> 3) * 4096), "i"(((i >> 1) & 3) * 1024) : "memory");
#pragma unroll
    for (int T = 0; T < 3; ++T) asm volatile("global_load_dwordx2 %0, %1, %2 sc1" : "=v"(res[T]) : "v"(ra[T]), "s"(ws) : "memory");
  }
  f32x4 pa[2][5] = {};
#pragma unroll
  for (int i = 0; i < 10; ++i) {
    WAITV(12 - i);
    PIN(of[i]);
    SBAR();
#pragma unroll
    for (int T = 0; T < 5; ++T) {
      const int f = (i >> 1) * 5 + T, e = (f & 7) * 4;
      const u32x32& wv = f < 8 ? wf0 : f < 16 ? wf1 : wf2;
      const u32x4 wfr = f == 24 ? wf3 : (u32x4){wv[e], wv[e + 1], wv[e + 2], wv[e + 3]};
      pa[i & 1][T] = MFMA16(__builtin_bit_cast(bf16x8, wfr), __builtin_bit_cast(bf16x8, of[i]), pa[i & 1][T]);
    }
    SBAR();
  }
  {
    const lds_ptr ex = smem + w * 8192 + l * 16;
#pragma unroll
    for (int i = 0; i < 8; ++i) *(LAS f32x4*)(ex + i * 1024) = i < 5 ? pa[1][i] : pa[0][i - 3];
    WAIT_BAR(0);
    const unsigned eb = lds0 + w * 8192 + l * 16;
#define EXR(d, i) (*(const LAS f32x4*)(uintptr_t)((eb ^ ((d) * 16384)) + (i) * 1024))
    f32x4 fin[3];
    fin[0] = (pa[0][0] + EXR(1, 0)) + (EXR(2, 6) + EXR(3, 3));
    fin[1] = (pa[0][1] + EXR(1, 1)) + (EXR(2, 7) + EXR(3, 4));
    fin[2] = (pa[0][2] + EXR(1, 2)) + (EXR(2, 5) + EXR(3, 2));
    float ssq = 0.f;
#pragma unroll
    for (int T = 0; T < 3; ++T) {
      PIN(res[T]);
      if (T == 2 && !early) break;
      const float v0 = fin[T][0] + lo16(res[T][0]), v1 = fin[T][1] + hi16(res[T][0]);
      const float v2 = fin[T][2] + lo16(res[T][1]), v3 = fin[T][3] + hi16(res[T][1]);
      if (LAYER) *(u32x2*)(out + oa[T]) = (u32x2){pack2(v0, v1), pack2(v2, v3)};
      else __builtin_amdgcn_raw_buffer_store_b64((u32x2){pack2(v0, v1), pack2(v2, v3)}, wsr, oa[T], 0, 16);
      ssq += v0 * v0 + ((T == 0 && early && cb + g == 0) ? 0.f : v1 * v1) + v2 * v2 + v3 * v3;
    }
    if (!LAYER) {
      ssq = sum32(sum16(ssq));
      if (g == 0) __hip_atomic_fetch_add((GAS double*)(ws + OFF_SS2) + srow, (double)ssq, RLX_AGENT);
    }
  }
}

__global__ __launch_bounds__(512, 2) void k_main(const char* __restrict__ x, const char* __restrict__ wr, const char* __restrict__ wi,
                                                 const char* __restrict__ wqkv, const char* __restrict__ wo, char* __restrict__ ws,
                                                 char* __restrict__ out) {
  extern __shared__ __attribute__((aligned(16))) char smem_[];
  lds_ptr smem = (lds_ptr)smem_;
  {
    const int bid = BIDX, tid = TIDX;
    asm volatile("" ::"s"(x), "s"(wr), "s"(wi), "s"(wqkv), "s"(wo), "s"(ws));
    if (__builtin_expect(bid < 160 && uni(tid) < 256, 1)) {
#pragma unroll 1
      for (int i = 0; i < 3; ++i) dft_stage<false>(x, ((bid & 7) * 4 + (bid >> 3) / 5) * 64, (unsigned)(uintptr_t)smem, uni(tid >> 6), tid & 63, i);
    }
    if ((uni(tid) & ~64) == 256) {
      unsigned long long pc;
      asm volatile("s_getpc_b64 s[40:41]\n\ts_add_u32 s40, s40, _Z6k_mainPKcS0_S0_S0_S0_PcS1_@rel32@lo+4\n\t"
                   "s_addc_u32 s41, s41, _Z6k_mainPKcS0_S0_S0_S0_PcS1_@rel32@hi+12" : "={s[40:41]}"(pc)::"scc");
      glds16s((const char*)pc, imin(((bid >> 3) * 128 + (tid & 127)) * 16, CODE_B - 16), (unsigned)(uintptr_t)smem + (bid < 160 ? 126976 : 151552));
    }
    asm volatile("" ::: "memory");
    if (__builtin_expect(bid < 160, 1)) {
      pre_rows(x, wr, wi, ws, smem, bid, tid);
      gsignal(ws, 0, bid, tid);
    } else {
      gu32* bea = (gu32*)(ws + OFF_BEA);
      const int j = (bid & 7) * 12 + ((bid - 160) >> 3), w = uni(tid >> 6);
      unsigned b0 = 0;
      if (j == 94) {
        const __amdgpu_buffer_rsrc_t zr = __builtin_amdgcn_make_buffer_rsrc(ws + OFF_GC, 0, 131072, 0x00020000);
        __builtin_amdgcn_raw_buffer_store_b32(0u, zr, tid * 128, 0, 16);
        __builtin_amdgcn_raw_buffer_store_b32(0u, zr, tid * 128, 65536, 16);
        WAIT_BAR(0);
        if (w == 0) __hip_atomic_fetch_add(bea + tid * 32, 1u, RLX_AGENT);
      } else if (w == 7) {
        b0 = uni(__hip_atomic_load(bea + (bid & 63) * 32, RLX_AGENT));
      }
      pre_wqkv(wqkv, ws, smem, j, tid);
      if (j != 94 && w == 7) beacon_wait(bea + (bid & 63) * 32, b0);
      gsignal(ws, 1, bid, tid);
      if (j == 94 && w == 0) {
        gu32* c = (gu32*)(ws + OFF_GC) + (((tid >> 3) & 1) * 512 + (bid & 63) * 8 + (tid & 7)) * 32;
#pragma unroll 1
        for (unsigned spins = 0; spins < SPIN_MAX; ++spins) {
          __hip_atomic_fetch_add(bea + tid * 32, 1u, RLX_AGENT);
          if (!wany(__hip_atomic_load(c, RLX_AGENT) < ((tid & 8) ? 12u : 20u))) break;
          __builtin_amdgcn_s_sleep(8);
        }
      }
      pre_wo(wo, ws, smem, j, tid);
      if (w == 0) { gpoll(ws, 0, 20, bid, tid); gpoll(ws, 1, 12, bid, tid); }
      gsignal(ws, 2, bid, tid);
    }
  }
#pragma unroll 1
  for (int layer = 0; layer < 2; ++layer) {
    int bid = BIDX, tid = TIDX;
    asm volatile("" : "+s"(bid), "+v"(tid));
    if (bid < 160) {
      col_phase(ws, smem, layer, bid, tid);
      if (layer == 0 && uni(tid) < 64) gpoll(ws, 1, 12, bid, tid);
      gsignal(ws, 2 + 2 * layer, bid, tid);
    }
    asm volatile("" : "+s"(bid), "+v"(tid));
    attn_phase(ws, out, smem, layer, bid, tid);
    if (layer == 0) gsignal(ws, 3, bid, tid);
  }
}

#if defined(__HIP_DEVICE_COMPILE__) && __HIP_DEVICE_COMPILE__
#pragma clang attribute pop
#endif

extern "C" void kernel_launch(void* const* d_in, const int* in_sizes, int n_in, void* d_out, int out_size, void* d_ws, size_t ws_size,
                              hipStream_t stream) {
  const char* x = (const char*)d_in[0];
  const char* wr = (const char*)d_in[1];
  const char* wi = (const char*)d_in[2];
  const char* wqkv = (const char*)d_in[4];
  const char* wo = (const char*)d_in[5];
  char* ws = (char*)d_ws;
  (void)hipFuncSetAttribute((const void*)k_main, hipFuncAttributeMaxDynamicSharedMemorySize, ATT_LDS);
  k_main<<<256, 512, ATT_LDS, stream>>>(x, wr, wi, wqkv, wo, ws, (char*)d_out);
}
```
